```python
import torch

def _softmax(x, axis=-1):
    return torch.softmax(x, dim=axis)
def _gelu(x):
    return torch.nn.functional.gelu(x, approximate="tanh")
def _silu(x):
    return torch.nn.functional.silu(x)
def _sigmoid(x):
    return torch.sigmoid(x)
def _layernorm(x, axis=-1, eps=1e-5):
    m = x.mean(dim=axis, keepdim=True)
    v = x.var(dim=axis, keepdim=True, unbiased=False)
    return (x - m) / torch.sqrt(v + eps)
def _scatter_add(tgt, idx, src, axis=0):
    return tgt.clone().index_add_(axis, idx, src)
def _segment_sum(src, seg, n):
    out = torch.zeros((n,) + src.shape[1:], dtype=src.dtype)
    return out.index_add_(0, seg, src)
def _pad(x, pw):
    flat = tuple(v for pair in reversed(pw) for v in pair)
    return torch.nn.functional.pad(x, flat)

kernel_name = 'gated_mix__mha_block_cached__glu_gated_geglu__decode'

def setup_inputs():

    _w = (lambda a: (lambda t: t.to(torch.bfloat16) if t.is_floating_point() else t)(torch.as_tensor(a)))
    def _A_setup():
        B, Sq, D = 4, 1, 640
        H, Sk, Dh = 32, 8192, 192
        return {
            "x": _w((lambda *s: torch.randn(*s, dtype=torch.bfloat16) * 0.05)(B, Sq, D)),
            "k_cache": _w((lambda *s: torch.randn(*s, dtype=torch.bfloat16) * 3.2)(B, H, Sk, Dh)),
            "v_cache": _w((lambda *s: torch.randn(*s, dtype=torch.bfloat16) * 0.05)(B, H, Sk, Dh)),
            "wq": _w((lambda *s: torch.randn(*s, dtype=torch.bfloat16) * 3.2)(D, H * Dh) * (D ** -0.5)),
            "wo": _w((lambda *s: torch.randn(*s, dtype=torch.bfloat16) * 0.05)(H * Dh, D) * ((H * Dh) ** -0.5)),
        }
    _A_in = _A_setup()

    def _B_setup():
        B, S, Dm, Df = 4, 1, 640, 1536
        return {
            "x": _w((lambda *s: torch.randn(*s, dtype=torch.bfloat16) * 0.05)(B, S, Dm)),
            "w_gate": _w((lambda *s: torch.randn(*s, dtype=torch.bfloat16) * 0.2)(Dm, Df) * (Dm ** -0.5)),
            "w_up": _w((lambda *s: torch.randn(*s, dtype=torch.bfloat16) * 0.2)(Dm, Df) * (Dm ** -0.5)),
            "w_down": _w((lambda *s: torch.randn(*s, dtype=torch.bfloat16) * 0.2)(Df, Dm) * (Df ** -0.5)),
        }
    _B_in = _B_setup()

    return {
        "A_x": _A_in["x"],
        "A_k_cache": _A_in["k_cache"],
        "A_v_cache": _A_in["v_cache"],
        "A_wq": _A_in["wq"],
        "A_wo": _A_in["wo"],
        "B_x": _B_in["x"],
        "B_w_gate": _B_in["w_gate"],
        "B_w_up": _B_in["w_up"],
        "B_w_down": _B_in["w_down"],
        "gate_w": _w((lambda *s: torch.randn(*s, dtype=torch.bfloat16) * 0.05)(640) * 16.0),
    }

def reference(A_x, A_k_cache, A_v_cache, A_wq, A_wo, B_x, B_w_gate, B_w_up, B_w_down, gate_w):
    def _A_ref(x, k_cache, v_cache, wq, wo):
        B, Sq, D = x.shape
        H, Dh = 32, 192
        q = (lambda x, *d: x.permute(*d))((x @ wq).reshape(B, Sq, H, Dh), 0, 2, 1, 3)
        scores = torch.einsum("bhqd,bhkd->bhqk", q, k_cache) * (Dh ** -0.5)
        p = _softmax(scores, axis=-1)
        o = torch.einsum("bhqk,bhkd->bhqd", p, v_cache)
        return (lambda x, *d: x.permute(*d))(o, 0, 2, 1, 3).reshape(B, Sq, H * Dh) @ wo

    def _B_ref(x, w_gate, w_up, w_down):
        g = torch.einsum("bsd,df->bsf", x, w_gate)
        u = torch.einsum("bsd,df->bsf", x, w_up)
        h = _gelu(g) * u
        return torch.einsum("bsf,fd->bsd", h, w_down)

    x = A_x
    g = _sigmoid((x * gate_w).sum(-1, keepdims=True))
    a = _A_ref(x, A_k_cache, A_v_cache, A_wq, A_wo)
    b = _B_ref(x, B_w_gate, B_w_up, B_w_down)
    return g * a + (1 - g) * b
```

```cpp
#include <hip/hip_runtime.h>
#include <hip/hip_bf16.h>
#include <stdint.h>

#define GAS __attribute__((address_space(1)))
#define LAS __attribute__((address_space(3)))
typedef unsigned u32x4 __attribute__((ext_vector_type(4)));
typedef unsigned u32x2 __attribute__((ext_vector_type(2)));
typedef float f32x4 __attribute__((ext_vector_type(4)));
typedef float f32x2 __attribute__((ext_vector_type(2)));
typedef __bf16 bf16x2 __attribute__((ext_vector_type(2)));

namespace {
constexpr int B = 4, H = 32, SK = 8192, DH = 192, D = 640, DF = 1536;
constexpr int NW = 8;
constexpr int NT = NW * 64;
constexpr int KEYS_WG = SK / 2;
constexpr int ROWB = DH * 2;
constexpr int GRP_B = 8 * ROWB;
constexpr int NGRP = KEYS_WG / 8;
constexpr int NBUF = 2;
constexpr int WS_Y = 0;
constexpr int WS_F = 256 * 640;
constexpr int WS_ML = 2 * 256 * 640;
constexpr int AUX_NT = 2;

__device__ __forceinline__ float dot2(unsigned a, unsigned b, float acc) {
  return __builtin_amdgcn_fdot2_f32_bf16(__builtin_bit_cast(bf16x2, a), __builtin_bit_cast(bf16x2, b), acc, false);
}
template <int CTRL> __device__ __forceinline__ float dpp(float x) {
  return __builtin_bit_cast(float, __builtin_amdgcn_mov_dpp(__builtin_bit_cast(int, x), CTRL, 0xf, 0xf, true));
}
constexpr int XOR1 = 0xB1, XOR2 = 0x4E, HMIR = 0x141, ROR8 = 0x128;
__device__ __forceinline__ float bflo(unsigned u) { return __builtin_bit_cast(float, u << 16); }
__device__ __forceinline__ float bfhi(unsigned u) { return __builtin_bit_cast(float, u & 0xffff0000u); }
__device__ __forceinline__ float bf2f(unsigned short u) { return __builtin_bit_cast(float, (unsigned)u << 16); }
__device__ __forceinline__ unsigned short f2bf(float f) { return __builtin_bit_cast(unsigned short, (__bf16)f); }
__device__ __forceinline__ float rbf(float f) { return bf2f(f2bf(f)); }
__device__ __forceinline__ unsigned pk2(float lo, float hi) { return (unsigned)f2bf(lo) | ((unsigned)f2bf(hi) << 16); }
__device__ __forceinline__ float swap32_add(float a, float b) {
  auto r = __builtin_amdgcn_permlane32_swap(__builtin_bit_cast(unsigned, a), __builtin_bit_cast(unsigned, b), false, false);
  return __uint_as_float(r[0]) + __uint_as_float(r[1]);
}
__device__ __forceinline__ float swap16_add(float a, float b) {
  auto r = __builtin_amdgcn_permlane16_swap(__builtin_bit_cast(unsigned, a), __builtin_bit_cast(unsigned, b), false, false);
  return __uint_as_float(r[0]) + __uint_as_float(r[1]);
}
__device__ __forceinline__ float xmax_8_16_32(float x) {
  x = fmaxf(x, dpp<ROR8>(x));
  auto s = __builtin_amdgcn_permlane16_swap(__builtin_bit_cast(unsigned, x), __builtin_bit_cast(unsigned, x), false, false);
  x = fmaxf(__uint_as_float(s[0]), __uint_as_float(s[1]));
  auto t = __builtin_amdgcn_permlane32_swap(__builtin_bit_cast(unsigned, x), __builtin_bit_cast(unsigned, x), false, false);
  return fmaxf(__uint_as_float(t[0]), __uint_as_float(t[1]));
}
__device__ __forceinline__ float xsum_8_16_32(float x) {
  x = x + dpp<ROR8>(x);
  x = swap16_add(x, x);
  return swap32_add(x, x);
}
template <int N> __device__ __forceinline__ void slot_reduce(float (&v)[N], int lane) {
#pragma unroll
  for (int j = 0; j < N / 2; ++j) v[j] = swap32_add(v[j], v[j + N / 2]);
#pragma unroll
  for (int j = 0; j < N / 4; ++j) v[j] = swap16_add(v[j], v[j + N / 4]);
#pragma unroll
  for (int j = 0; j < N / 8; ++j) {
    const float a = v[j] + dpp<ROR8>(v[j]), b = v[j + N / 8] + dpp<ROR8>(v[j + N / 8]);
    v[j] = (lane & 8) ? b : a;
  }
}
__device__ __forceinline__ u32x4 bload(__amdgpu_buffer_rsrc_t r, int voff, int aux_nt) {
  return aux_nt ? __builtin_amdgcn_raw_buffer_load_b128(r, voff, 0, AUX_NT) : __builtin_amdgcn_raw_buffer_load_b128(r, voff, 0, 0);
}
__device__ __forceinline__ __amdgpu_buffer_rsrc_t mkrsrc(const void* p, unsigned bytes) {
  return __builtin_amdgcn_make_buffer_rsrc((void*)p, 0, (int)bytes, 0x00020000);
}

constexpr int L_RED = 0;
constexpr int L_Q = NW * 4 * 640;
constexpr int L_H = L_Q + 96;
constexpr int L_O = L_H + 32;
constexpr int L_ML = L_O + NW * 192;
constexpr int L_OP = L_ML + 16;
constexpr int L_END = L_OP + NW * 16;

__global__ void __launch_bounds__(NT, 2)
k1_attn(const unsigned short* __restrict__ x, const unsigned short* __restrict__ kc, const unsigned short* __restrict__ vc,
        const unsigned short* __restrict__ wq, const unsigned short* __restrict__ wo, const unsigned short* __restrict__ wg,
        const unsigned short* __restrict__ wu, const unsigned short* __restrict__ wd, float* __restrict__ ws) {
  __shared__ __attribute__((aligned(16))) float lds[L_END];
  const int tid = threadIdx.x, lane = tid & 63, w = __builtin_amdgcn_readfirstlane(tid >> 6);
  const int rs = lane >> 3, c8 = lane & 7;
  const int bid = blockIdx.x, xg = bid & 7, j = bid >> 3;
  const int hl = j & 3, h = xg * 4 + hl, b = j >> 3, s = (j >> 2) & 1;
  const int f0 = xg * 192 + (hl * 2 + s) * 24;
  const int slot = b * 64 + h * 2 + s;

  float xv[10];
#pragma unroll
  for (int g = 0; g < 10; ++g) xv[g] = bf2f(x[b * D + 80 * w + 8 * g + rs]);
  const __amdgpu_buffer_rsrc_t rq = mkrsrc(wq, D * H * DH * 2);
  const int qoff = (80 * w + rs) * (H * DH * 2) + h * ROWB + c8 * 16;
  u32x4 wr[10][3];
#pragma unroll
  for (int g = 0; g < 10; ++g)
#pragma unroll
    for (int i = 0; i < 3; ++i) wr[g][i] = bload(rq, qoff + g * 8 * (H * DH * 2) + i * 128, 0);
  __builtin_amdgcn_sched_barrier(0);
  const size_t kvbase = ((size_t)(b * H + h) * SK + (size_t)s * KEYS_WG) * DH;
  const __amdgpu_buffer_rsrc_t rk = mkrsrc(kc + kvbase, KEYS_WG * ROWB);
  const __amdgpu_buffer_rsrc_t rv = mkrsrc(vc + kvbase, KEYS_WG * ROWB);
  const int loff = rs * ROWB + c8 * 16;
  u32x4 kb[NBUF][3], vb[NBUF][3];
#pragma unroll
  for (int u = 0; u < NBUF; ++u) {
    const int o = loff + (u * NW + w) * GRP_B;
#pragma unroll
    for (int i = 0; i < 3; ++i) { kb[u][i] = bload(rk, o + i * 128, 1); __builtin_amdgcn_sched_barrier(0); }
#pragma unroll
    for (int i = 0; i < 3; ++i) { vb[u][i] = bload(rv, o + i * 128, 1); __builtin_amdgcn_sched_barrier(0); }
  }
  const int cc = c8 >= 6 ? c8 - 6 : c8;
  const unsigned short* gp = (cc < 3 ? wg : wu) + (size_t)(80 * w + rs) * DF + f0 + (cc < 3 ? cc : cc - 3) * 8;
  u32x4 gr[10];
#pragma unroll
  for (int g = 0; g < 10; ++g) gr[g] = *(const GAS u32x4*)(gp + (size_t)g * 8 * DF);
  __builtin_amdgcn_sched_barrier(0);

  {
    float a[32];
#pragma unroll
    for (int k = 0; k < 32; ++k) a[k] = 0.f;
#pragma unroll
    for (int g = 0; g < 10; ++g) {
#pragma unroll
      for (int i = 0; i < 3; ++i)
#pragma unroll
        for (int e = 0; e < 4; ++e) {
          a[i * 8 + 2 * e] = fmaf(xv[g], bflo(wr[g][i][e]), a[i * 8 + 2 * e]);
          a[i * 8 + 2 * e + 1] = fmaf(xv[g], bfhi(wr[g][i][e]), a[i * 8 + 2 * e + 1]);
        }
    }
#pragma unroll
    for (int g = 0; g < 10; ++g)
#pragma unroll
      for (int e = 0; e < 4; ++e) {
        a[24 + 2 * e] = fmaf(xv[g], bflo(gr[g][e]), a[24 + 2 * e]);
        a[24 + 2 * e + 1] = fmaf(xv[g], bfhi(gr[g][e]), a[24 + 2 * e + 1]);
      }
    slot_reduce<32>(a, lane);
    const int i = lane >> 4;
    *(LAS f32x4*)((LAS float*)lds + L_RED + w * 256 + i * 64 + c8 * 8 + ((lane >> 3) & 1) * 4) = f32x4{a[0], a[1], a[2], a[3]};
  }
  const __amdgpu_buffer_rsrc_t rd = mkrsrc(wd + (size_t)f0 * D, 24 * D * 2);
  u32x4 dr[5];
#pragma unroll
  for (int k = 0; k < 5; ++k) dr[k] = bload(rd, w * 5120 + k * 1024 + lane * 16, 0);
  const __amdgpu_buffer_rsrc_t ro = mkrsrc(wo + (size_t)h * DH * D, DH * D * 2);
  u32x4 wor[30];
#pragma unroll
  for (int i = 0; i < 30; ++i) wor[i] = bload(ro, w * 30720 + i * 1024 + lane * 16, 0);
  __builtin_amdgcn_sched_barrier(0);
  __syncthreads();
  if (tid < 128) {
    f32x2 t = {0.f, 0.f};
#pragma unroll
    for (int ww = 0; ww < NW; ++ww) t += *(LAS f32x2*)((LAS float*)lds + L_RED + ww * 256 + tid * 2);
    if (tid < 96) ((LAS unsigned*)lds)[L_Q + tid] = pk2(t.x, t.y);
    else *(LAS f32x2*)((LAS float*)lds + L_O + (tid - 96) * 2) = t;
  }
  __syncthreads();
  u32x4 qf[3];
#pragma unroll
  for (int i = 0; i < 3; ++i) qf[i] = *(LAS u32x4*)((LAS unsigned*)lds + L_Q + i * 32 + c8 * 4);
  {
    float fa[5][8];
#pragma unroll
    for (int k = 0; k < 5; ++k) {
      const int r = min(4 * w + (64 * k + lane) / 80, 23);
      const float g = rbf(lds[L_O + r]), u = rbf(lds[L_O + 24 + r]);
      const float inner = 0.7978845608028654f * (g + 0.044715f * g * g * g);
      const float th = 1.f - 2.f / (1.f + __expf(2.f * inner));
      const float hv = rbf(rbf(0.5f * g * (1.f + th)) * u);
#pragma unroll
      for (int e = 0; e < 4; ++e) { fa[k][2 * e] = hv * bflo(dr[k][e]); fa[k][2 * e + 1] = hv * bfhi(dr[k][e]); }
    }
#pragma unroll
    for (int k = 0; k < 5; ++k) {
      LAS f32x4* p = (LAS f32x4*)((LAS float*)lds + L_RED + w * 2560 + (64 * k + lane) * 8);
      p[0] = f32x4{fa[k][0], fa[k][1], fa[k][2], fa[k][3]};
      p[1] = f32x4{fa[k][4], fa[k][5], fa[k][6], fa[k][7]};
    }
    __syncthreads();
    if (tid < 160) {
      f32x4 t = {0.f, 0.f, 0.f, 0.f};
#pragma unroll
      for (int r = 0; r < 24; ++r) t += *(LAS f32x4*)((LAS float*)lds + L_RED + r * 640 + tid * 4);
      *(GAS f32x4*)(ws + WS_F + slot * 640 + tid * 4) = t;
    }
  }

  constexpr float C2 = 0.07216878364870322f * 1.4426950408889634f;
  float m = -INFINITY, l = 0.f, acc[24];
#pragma unroll
  for (int k = 0; k < 24; ++k) acc[k] = 0.f;
  int onext = loff + (NBUF * NW + w) * GRP_B;
  for (int it = 0; it < NGRP / NW / NBUF; ++it) {
#pragma unroll
    for (int u = 0; u < NBUF; ++u) {
      float s0 = 0.f, s1 = 0.f;
#pragma unroll
      for (int i = 0; i < 3; ++i) {
        s0 = dot2(kb[u][i][0], qf[i][0], s0); s1 = dot2(kb[u][i][1], qf[i][1], s1);
        s0 = dot2(kb[u][i][2], qf[i][2], s0); s1 = dot2(kb[u][i][3], qf[i][3], s1);
      }
      __builtin_amdgcn_sched_barrier(0);
#pragma unroll
      for (int i = 0; i < 3; ++i) { kb[u][i] = bload(rk, onext + i * 128, 1); __builtin_amdgcn_sched_barrier(0); }
      float sc = s0 + s1;
      sc += dpp<XOR1>(sc); sc += dpp<XOR2>(sc); sc += dpp<HMIR>(sc);
      sc *= C2;
      const float mn = fmaxf(m, sc);
      const float alpha = __builtin_amdgcn_exp2f(m - mn), p = __builtin_amdgcn_exp2f(sc - mn);
      m = mn;
      l = fmaf(l, alpha, p);
#pragma unroll
      for (int i = 0; i < 3; ++i)
#pragma unroll
        for (int e = 0; e < 4; ++e) {
          acc[i * 8 + 2 * e] = fmaf(acc[i * 8 + 2 * e], alpha, p * bflo(vb[u][i][e]));
          acc[i * 8 + 2 * e + 1] = fmaf(acc[i * 8 + 2 * e + 1], alpha, p * bfhi(vb[u][i][e]));
        }
      __builtin_amdgcn_sched_barrier(0);
#pragma unroll
      for (int i = 0; i < 3; ++i) { vb[u][i] = bload(rv, onext + i * 128, 1); __builtin_amdgcn_sched_barrier(0); }
      onext += NW * GRP_B;
    }
  }

  {
    const float M = xmax_8_16_32(m);
    const float sc = __builtin_amdgcn_exp2f(m - M);
    const float L = xsum_8_16_32(l * sc);
#pragma unroll
    for (int k = 0; k < 24; ++k) acc[k] *= sc;
    slot_reduce<24>(acc, lane);
    const int kbase = 3 * ((lane >> 3) & 1) + 6 * ((lane >> 4) & 1) + 12 * (lane >> 5);
#pragma unroll
    for (int jj = 0; jj < 3; ++jj) { const int k = kbase + jj; lds[L_O + w * 192 + (k >> 3) * 64 + c8 * 8 + (k & 7)] = acc[jj]; }
    if (lane == 0) { lds[L_ML + 2 * w] = M; lds[L_ML + 2 * w + 1] = L; }
  }
  __syncthreads();
  {
    float M = -INFINITY;
#pragma unroll
    for (int ww = 0; ww < NW; ++ww) M = fmaxf(M, lds[L_ML + 2 * ww]);
    float L = 0.f, o = 0.f;
    const int dd = 24 * w + min(lane, 23);
#pragma unroll
    for (int ww = 0; ww < NW; ++ww) {
      const float e = __builtin_amdgcn_exp2f(lds[L_ML + 2 * ww] - M);
      L = fmaf(e, lds[L_ML + 2 * ww + 1], L);
      o = fmaf(e, lds[L_O + ww * 192 + dd], o);
    }
    o *= 1.f / L;
    if (tid == 0) *(GAS f32x2*)(ws + WS_ML + slot * 2) = f32x2{M, L};
    const float o4 = __shfl(o, (lane + 4) & 63);
    if (lane < 24 && (lane & 4) == 0) ((LAS unsigned*)lds)[L_OP + w * 16 + (lane >> 3) * 4 + (lane & 3)] = pk2(o, o4);
    __builtin_amdgcn_fence(__ATOMIC_RELEASE, "wavefront");
    __builtin_amdgcn_wave_barrier();
    __builtin_amdgcn_fence(__ATOMIC_ACQUIRE, "wavefront");
    float ya[5][8];
#pragma unroll
    for (int k = 0; k < 5; ++k) {
      const int rr = (64 * k + lane) / 80;
#pragma unroll
      for (int e = 0; e < 8; ++e) ya[k][e] = 0.f;
#pragma unroll
      for (int p = 0; p < 3; ++p) {
        const unsigned op = ((LAS unsigned*)lds)[L_OP + w * 16 + p * 4 + rr];
        const u32x4 r0 = wor[10 * p + k], r1 = wor[10 * p + 5 + k];
#pragma unroll
        for (int e = 0; e < 4; ++e) {
          ya[k][2 * e] = dot2(__builtin_amdgcn_perm(r1[e], r0[e], 0x05040100u), op, ya[k][2 * e]);
          ya[k][2 * e + 1] = dot2(__builtin_amdgcn_perm(r1[e], r0[e], 0x07060302u), op, ya[k][2 * e + 1]);
        }
      }
    }
#pragma unroll
    for (int k = 0; k < 5; ++k) {
      LAS f32x4* p = (LAS f32x4*)((LAS float*)lds + L_RED + w * 2560 + (64 * k + lane) * 8);
      p[0] = f32x4{ya[k][0], ya[k][1], ya[k][2], ya[k][3]};
      p[1] = f32x4{ya[k][4], ya[k][5], ya[k][6], ya[k][7]};
    }
  }
  __syncthreads();
  if (tid < 320) {
    f32x2 t = {0.f, 0.f};
#pragma unroll
    for (int r = 0; r < 32; ++r) t += *(LAS f32x2*)((LAS float*)lds + L_RED + r * 640 + tid * 2);
    *(GAS f32x2*)(ws + WS_Y + slot * 640 + tid * 2) = t;
  }
}

__global__ void __launch_bounds__(64)
k2_mix(const unsigned short* __restrict__ x, const unsigned short* __restrict__ gw, const float* __restrict__ ws,
       unsigned short* __restrict__ out) {
  const int lane = threadIdx.x, b = blockIdx.x / 40, c0 = (blockIdx.x % 40) * 16;
  const int pc = lane & 3, v0 = lane >> 2;
  f32x4 y[4], f[4], ml[4];
#pragma unroll
  for (int i = 0; i < 4; ++i) {
    const int v = b * 64 + v0 + 16 * i;
    y[i] = *(const GAS f32x4*)(ws + WS_Y + v * 640 + c0 + pc * 4);
    f[i] = *(const GAS f32x4*)(ws + WS_F + v * 640 + c0 + pc * 4);
    ml[i] = *(const GAS f32x4*)(ws + WS_ML + (v & ~1) * 2);
  }
  const u32x4 xa = *(const GAS u32x4*)(x + b * D + lane * 8), ga = *(const GAS u32x4*)(gw + lane * 8);
  const int l2 = 64 + (lane & 15);
  const u32x4 xb = *(const GAS u32x4*)(x + b * D + l2 * 8), gb = *(const GAS u32x4*)(gw + l2 * 8);
  float gs = 0.f, gs2 = 0.f;
#pragma unroll
  for (int e = 0; e < 4; ++e) {
    gs += rbf(bflo(xa[e]) * bflo(ga[e])) + rbf(bfhi(xa[e]) * bfhi(ga[e]));
    gs2 += rbf(bflo(xb[e]) * bflo(gb[e])) + rbf(bfhi(xb[e]) * bfhi(gb[e]));
  }
  gs += lane < 16 ? gs2 : 0.f;
  gs += dpp<XOR1>(gs); gs += dpp<XOR2>(gs); gs += dpp<HMIR>(gs);
  gs = xsum_8_16_32(gs);
  const float g = rbf(1.f / (1.f + __expf(-rbf(gs))));
  f32x4 a = {0.f, 0.f, 0.f, 0.f}, bb = {0.f, 0.f, 0.f, 0.f};
#pragma unroll
  for (int i = 0; i < 4; ++i) {
    const float M = fmaxf(ml[i][0], ml[i][2]);
    const float w0 = __builtin_amdgcn_exp2f(ml[i][0] - M) * ml[i][1], w1 = __builtin_amdgcn_exp2f(ml[i][2] - M) * ml[i][3];
    const float wt = ((v0 & 1) ? w1 : w0) / (w0 + w1);
    a += wt * y[i];
    bb += f[i];
  }
  float r[8] = {a[0], a[1], a[2], a[3], bb[0], bb[1], bb[2], bb[3]};
#pragma unroll
  for (int k = 0; k < 8; ++k) {
    float t = r[k];
    t += __shfl_xor(t, 4);
    r[k] = xsum_8_16_32(t);
  }
  if (lane < 4) {
    float o[4];
#pragma unroll
    for (int k = 0; k < 4; ++k) o[k] = rbf(g * rbf(r[k])) + rbf(rbf(1.f - g) * rbf(r[4 + k]));
    *(GAS u32x2*)(out + b * D + c0 + pc * 4) = u32x2{pk2(o[0], o[1]), pk2(o[2], o[3])};
  }
}
}

extern "C" void kernel_launch(void* const* d_in, const int* in_sizes, int n_in, void* d_out, int out_size, void* d_ws,
                              size_t ws_size, hipStream_t stream) {
  const unsigned short* x = (const unsigned short*)d_in[0];
  const unsigned short* kc = (const unsigned short*)d_in[1];
  const unsigned short* vc = (const unsigned short*)d_in[2];
  const unsigned short* wq = (const unsigned short*)d_in[3];
  const unsigned short* wo = (const unsigned short*)d_in[4];
  const unsigned short* wg = (const unsigned short*)d_in[6];
  const unsigned short* wu = (const unsigned short*)d_in[7];
  const unsigned short* wd = (const unsigned short*)d_in[8];
  const unsigned short* gw = (const unsigned short*)d_in[9];
  float* ws = (float*)d_ws;
  hipLaunchKernelGGL(k1_attn, dim3(256), dim3(NT), 0, stream, x, kc, vc, wq, wo, wg, wu, wd, ws);
  hipLaunchKernelGGL(k2_mix, dim3(160), dim3(64), 0, stream, x, gw, (const float*)ws, (unsigned short*)d_out);
}
```

```cpp
#include <hip/hip_runtime.h>
#include <hip/hip_bf16.h>
#include <stdint.h>

#define GAS __attribute__((address_space(1)))
#define LAS __attribute__((address_space(3)))
typedef unsigned u32x4 __attribute__((ext_vector_type(4)));
typedef unsigned u32x2 __attribute__((ext_vector_type(2)));
typedef float f32x4 __attribute__((ext_vector_type(4)));
typedef float f32x2 __attribute__((ext_vector_type(2)));
typedef __bf16 bf16x2 __attribute__((ext_vector_type(2)));

namespace {
constexpr int B = 4, H = 32, SK = 8192, DH = 192, D = 640, DF = 1536;
constexpr int NW = 8;
constexpr int NT = NW * 64;
constexpr int KEYS_WG = SK / 2;
constexpr int ROWB = DH * 2;
constexpr int GRP_B = 8 * ROWB;
constexpr int NGRP = KEYS_WG / 8;
constexpr int NBUF = 2;
constexpr int WS_Y = 0;
constexpr int WS_F = 256 * 640;
constexpr int WS_ML = 2 * 256 * 640;
constexpr int AUX_NT = 2;

__device__ __forceinline__ float dot2(unsigned a, unsigned b, float acc) {
  return __builtin_amdgcn_fdot2_f32_bf16(__builtin_bit_cast(bf16x2, a), __builtin_bit_cast(bf16x2, b), acc, false);
}
template <int CTRL> __device__ __forceinline__ float dpp(float x) {
  return __builtin_bit_cast(float, __builtin_amdgcn_mov_dpp(__builtin_bit_cast(int, x), CTRL, 0xf, 0xf, true));
}
constexpr int XOR1 = 0xB1, XOR2 = 0x4E, HMIR = 0x141, ROR8 = 0x128;
__device__ __forceinline__ float bflo(unsigned u) { return __builtin_bit_cast(float, u << 16); }
__device__ __forceinline__ float bfhi(unsigned u) { return __builtin_bit_cast(float, u & 0xffff0000u); }
__device__ __forceinline__ float bf2f(unsigned short u) { return __builtin_bit_cast(float, (unsigned)u << 16); }
__device__ __forceinline__ unsigned short f2bf(float f) { return __builtin_bit_cast(unsigned short, (__bf16)f); }
__device__ __forceinline__ float rbf(float f) { return bf2f(f2bf(f)); }
__device__ __forceinline__ unsigned pk2(float lo, float hi) { return (unsigned)f2bf(lo) | ((unsigned)f2bf(hi) << 16); }
__device__ __forceinline__ float swap32_add(float a, float b) {
  auto r = __builtin_amdgcn_permlane32_swap(__builtin_bit_cast(unsigned, a), __builtin_bit_cast(unsigned, b), false, false);
  return __uint_as_float(r[0]) + __uint_as_float(r[1]);
}
__device__ __forceinline__ float swap16_add(float a, float b) {
  auto r = __builtin_amdgcn_permlane16_swap(__builtin_bit_cast(unsigned, a), __builtin_bit_cast(unsigned, b), false, false);
  return __uint_as_float(r[0]) + __uint_as_float(r[1]);
}
__device__ __forceinline__ float xmax_8_16_32(float x) {
  x = fmaxf(x, dpp<ROR8>(x));
  auto s = __builtin_amdgcn_permlane16_swap(__builtin_bit_cast(unsigned, x), __builtin_bit_cast(unsigned, x), false, false);
  x = fmaxf(__uint_as_float(s[0]), __uint_as_float(s[1]));
  auto t = __builtin_amdgcn_permlane32_swap(__builtin_bit_cast(unsigned, x), __builtin_bit_cast(unsigned, x), false, false);
  return fmaxf(__uint_as_float(t[0]), __uint_as_float(t[1]));
}
__device__ __forceinline__ float xsum_8_16_32(float x) {
  x = x + dpp<ROR8>(x);
  x = swap16_add(x, x);
  return swap32_add(x, x);
}
template <int N> __device__ __forceinline__ void slot_reduce(float (&v)[N], int lane) {
#pragma unroll
  for (int j = 0; j < N / 2; ++j) v[j] = swap32_add(v[j], v[j + N / 2]);
#pragma unroll
  for (int j = 0; j < N / 4; ++j) v[j] = swap16_add(v[j], v[j + N / 4]);
#pragma unroll
  for (int j = 0; j < N / 8; ++j) {
    const float a = v[j] + dpp<ROR8>(v[j]), b = v[j + N / 8] + dpp<ROR8>(v[j + N / 8]);
    v[j] = (lane & 8) ? b : a;
  }
}
__device__ __forceinline__ u32x4 bload(__amdgpu_buffer_rsrc_t r, int voff, int aux_nt) {
  return aux_nt ? __builtin_amdgcn_raw_buffer_load_b128(r, voff, 0, AUX_NT) : __builtin_amdgcn_raw_buffer_load_b128(r, voff, 0, 0);
}
__device__ __forceinline__ __amdgpu_buffer_rsrc_t mkrsrc(const void* p, unsigned bytes) {
  return __builtin_amdgcn_make_buffer_rsrc((void*)p, 0, (int)bytes, 0x00020000);
}

#define PIN24(a) asm volatile("" : "+v"(a[0]), "+v"(a[1]), "+v"(a[2]), "+v"(a[3]), "+v"(a[4]), "+v"(a[5]), "+v"(a[6]), "+v"(a[7]), \
  "+v"(a[8]), "+v"(a[9]), "+v"(a[10]), "+v"(a[11]), "+v"(a[12]), "+v"(a[13]), "+v"(a[14]), "+v"(a[15]), \
  "+v"(a[16]), "+v"(a[17]), "+v"(a[18]), "+v"(a[19]), "+v"(a[20]), "+v"(a[21]), "+v"(a[22]), "+v"(a[23]) :: "memory")

#define STAMP(i)
constexpr int L_RED = 0;
constexpr int L_Q = NW * 4 * 640;
constexpr int L_H = L_Q + 96;
constexpr int L_O = L_H + 32;
constexpr int L_ML = L_O + NW * 192;
constexpr int L_OP = L_ML + 16;
constexpr int L_END = L_OP + NW * 16;

__global__ void __launch_bounds__(NT, 2)
k1_attn(const unsigned short* __restrict__ x, const unsigned short* __restrict__ kc, const unsigned short* __restrict__ vc,
        const unsigned short* __restrict__ wq, const unsigned short* __restrict__ wo, const unsigned short* __restrict__ wg,
        const unsigned short* __restrict__ wu, const unsigned short* __restrict__ wd, float* __restrict__ ws) {
  __shared__ __attribute__((aligned(16))) float lds[L_END];
  const int tid = threadIdx.x, lane = tid & 63, w = __builtin_amdgcn_readfirstlane(tid >> 6);
  const int rs = lane >> 3, c8 = lane & 7;
  const int bid = blockIdx.x, xg = bid & 7, j = bid >> 3;
  const int hl = j & 3, h = xg * 4 + hl, b = j >> 3, s = (j >> 2) & 1;
  const int f0 = xg * 192 + (hl * 2 + s) * 24;
  const int slot = b * 64 + h * 2 + s;

  STAMP(0);
  float xv[10];
#pragma unroll
  for (int g = 0; g < 10; ++g) xv[g] = bf2f(x[b * D + 80 * w + 8 * g + rs]);
  const __amdgpu_buffer_rsrc_t rq = mkrsrc(wq, D * H * DH * 2);
  const int qoff = (80 * w + rs) * (H * DH * 2) + h * ROWB + c8 * 16;
  u32x4 wr[10][3];
#pragma unroll
  for (int g = 0; g < 10; ++g)
#pragma unroll
    for (int i = 0; i < 3; ++i) wr[g][i] = bload(rq, qoff + g * 8 * (H * DH * 2) + i * 128, 0);
  __builtin_amdgcn_sched_barrier(0);
  const size_t kvbase = ((size_t)(b * H + h) * SK + (size_t)s * KEYS_WG) * DH;
  const __amdgpu_buffer_rsrc_t rk = mkrsrc(kc + kvbase, KEYS_WG * ROWB);
  const __amdgpu_buffer_rsrc_t rv = mkrsrc(vc + kvbase, KEYS_WG * ROWB);
  const int loff = rs * ROWB + c8 * 16;
  u32x4 kb[NBUF][3], vb[NBUF][3];
#pragma unroll
  for (int u = 0; u < NBUF; ++u) {
    const int o = loff + (u * NW + w) * GRP_B;
#pragma unroll
    for (int i = 0; i < 3; ++i) { kb[u][i] = bload(rk, o + i * 128, 1); __builtin_amdgcn_sched_barrier(0); }
#pragma unroll
    for (int i = 0; i < 3; ++i) { vb[u][i] = bload(rv, o + i * 128, 1); __builtin_amdgcn_sched_barrier(0); }
  }
  const int cc = c8 >= 6 ? c8 - 6 : c8;
  const unsigned short* gp = (cc < 3 ? wg : wu) + (size_t)(80 * w + rs) * DF + f0 + (cc < 3 ? cc : cc - 3) * 8;
  u32x4 gr[10];
#pragma unroll
  for (int g = 0; g < 10; ++g) gr[g] = *(const GAS u32x4*)(gp + (size_t)g * 8 * DF);
  __builtin_amdgcn_sched_barrier(0);

  {
    float a[32];
#pragma unroll
    for (int k = 0; k < 32; ++k) a[k] = 0.f;
#pragma unroll
    for (int g = 0; g < 10; ++g) {
#pragma unroll
      for (int i = 0; i < 3; ++i)
#pragma unroll
        for (int e = 0; e < 4; ++e) {
          a[i * 8 + 2 * e] = fmaf(xv[g], bflo(wr[g][i][e]), a[i * 8 + 2 * e]);
          a[i * 8 + 2 * e + 1] = fmaf(xv[g], bfhi(wr[g][i][e]), a[i * 8 + 2 * e + 1]);
        }
    }
#pragma unroll
    for (int g = 0; g < 10; ++g)
#pragma unroll
      for (int e = 0; e < 4; ++e) {
        a[24 + 2 * e] = fmaf(xv[g], bflo(gr[g][e]), a[24 + 2 * e]);
        a[24 + 2 * e + 1] = fmaf(xv[g], bfhi(gr[g][e]), a[24 + 2 * e + 1]);
      }
    slot_reduce<32>(a, lane);
    const int i = lane >> 4;
    *(LAS f32x4*)((LAS float*)lds + L_RED + w * 256 + i * 64 + c8 * 8 + ((lane >> 3) & 1) * 4) = f32x4{a[0], a[1], a[2], a[3]};
  }
  STAMP(1);
  const __amdgpu_buffer_rsrc_t rd = mkrsrc(wd + (size_t)f0 * D, 24 * D * 2);
  u32x4 dr[5];
#pragma unroll
  for (int k = 0; k < 5; ++k) dr[k] = bload(rd, w * 5120 + k * 1024 + lane * 16, 0);
  const __amdgpu_buffer_rsrc_t ro = mkrsrc(wo + (size_t)h * DH * D, DH * D * 2);
  u32x4 wor[30];
#pragma unroll
  for (int i = 0; i < 30; ++i) wor[i] = bload(ro, w * 30720 + i * 1024 + lane * 16, 0);
  __builtin_amdgcn_sched_barrier(0);
  __syncthreads();
  if (tid < 128) {
    f32x2 t = {0.f, 0.f};
#pragma unroll
    for (int ww = 0; ww < NW; ++ww) t += *(LAS f32x2*)((LAS float*)lds + L_RED + ww * 256 + tid * 2);
    if (tid < 96) ((LAS unsigned*)lds)[L_Q + tid] = pk2(t.x, t.y);
    else *(LAS f32x2*)((LAS float*)lds + L_O + (tid - 96) * 2) = t;
  }
  __syncthreads();
  u32x4 qf[3];
#pragma unroll
  for (int i = 0; i < 3; ++i) qf[i] = *(LAS u32x4*)((LAS unsigned*)lds + L_Q + i * 32 + c8 * 4);
  STAMP(2);
  {
    float fa[5][8];
#pragma unroll
    for (int k = 0; k < 5; ++k) {
      const int r = min(4 * w + (64 * k + lane) / 80, 23);
      const float g = rbf(lds[L_O + r]), u = rbf(lds[L_O + 24 + r]);
      const float inner = 0.7978845608028654f * (g + 0.044715f * g * g * g);
      const float th = 1.f - 2.f / (1.f + __expf(2.f * inner));
      const float hv = rbf(rbf(0.5f * g * (1.f + th)) * u);
#pragma unroll
      for (int e = 0; e < 4; ++e) { fa[k][2 * e] = hv * bflo(dr[k][e]); fa[k][2 * e + 1] = hv * bfhi(dr[k][e]); }
    }
#pragma unroll
    for (int k = 0; k < 5; ++k) {
      LAS f32x4* p = (LAS f32x4*)((LAS float*)lds + L_RED + w * 2560 + (64 * k + lane) * 8);
      p[0] = f32x4{fa[k][0], fa[k][1], fa[k][2], fa[k][3]};
      p[1] = f32x4{fa[k][4], fa[k][5], fa[k][6], fa[k][7]};
    }
    __syncthreads();
    if (tid < 160) {
      f32x4 t = {0.f, 0.f, 0.f, 0.f};
#pragma unroll
      for (int r = 0; r < 24; ++r) t += *(LAS f32x4*)((LAS float*)lds + L_RED + r * 640 + tid * 4);
      *(GAS f32x4*)(ws + WS_F + slot * 640 + tid * 4) = t;
    }
  }

  STAMP(3);
  constexpr float C2 = 0.07216878364870322f * 1.4426950408889634f;
  float m = -INFINITY, l = 0.f, acc[24];
#pragma unroll
  for (int k = 0; k < 24; ++k) acc[k] = 0.f;
  int onext = loff + (NBUF * NW + w) * GRP_B;
  for (int it = 0; it < NGRP / NW / NBUF; ++it) {
#pragma unroll
    for (int u = 0; u < NBUF; ++u) {
      float s0 = 0.f, s1 = 0.f;
#pragma unroll
      for (int i = 0; i < 3; ++i) {
        s0 = dot2(kb[u][i][0], qf[i][0], s0); s1 = dot2(kb[u][i][1], qf[i][1], s1);
        s0 = dot2(kb[u][i][2], qf[i][2], s0); s1 = dot2(kb[u][i][3], qf[i][3], s1);
      }
      asm volatile("" : "+v"(s0), "+v"(s1) :: "memory");
      __builtin_amdgcn_sched_barrier(0);
#pragma unroll
      for (int i = 0; i < 3; ++i) { kb[u][i] = bload(rk, onext + i * 128, 1); __builtin_amdgcn_sched_barrier(0); }
      float sc = s0 + s1;
      sc += dpp<XOR1>(sc); sc += dpp<XOR2>(sc); sc += dpp<HMIR>(sc);
      sc *= C2;
      const float mn = fmaxf(m, sc);
      const float alpha = __builtin_amdgcn_exp2f(m - mn), p = __builtin_amdgcn_exp2f(sc - mn);
      m = mn;
      l = fmaf(l, alpha, p);
#pragma unroll
      for (int i = 0; i < 3; ++i)
#pragma unroll
        for (int e = 0; e < 4; ++e) {
          acc[i * 8 + 2 * e] = fmaf(acc[i * 8 + 2 * e], alpha, p * bflo(vb[u][i][e]));
          acc[i * 8 + 2 * e + 1] = fmaf(acc[i * 8 + 2 * e + 1], alpha, p * bfhi(vb[u][i][e]));
        }
      PIN24(acc);
      __builtin_amdgcn_sched_barrier(0);
#pragma unroll
      for (int i = 0; i < 3; ++i) { vb[u][i] = bload(rv, onext + i * 128, 1); __builtin_amdgcn_sched_barrier(0); }
      onext += NW * GRP_B;
    }
  }

  STAMP(4);
  {
    const float M = xmax_8_16_32(m);
    const float sc = __builtin_amdgcn_exp2f(m - M);
    const float L = xsum_8_16_32(l * sc);
#pragma unroll
    for (int k = 0; k < 24; ++k) acc[k] *= sc;
    slot_reduce<24>(acc, lane);
    const int kbase = 3 * ((lane >> 3) & 1) + 6 * ((lane >> 4) & 1) + 12 * (lane >> 5);
#pragma unroll
    for (int jj = 0; jj < 3; ++jj) { const int k = kbase + jj; lds[L_O + w * 192 + (k >> 3) * 64 + c8 * 8 + (k & 7)] = acc[jj]; }
    if (lane == 0) { lds[L_ML + 2 * w] = M; lds[L_ML + 2 * w + 1] = L; }
  }
  __syncthreads();
  STAMP(5);
  {
    float M = -INFINITY;
#pragma unroll
    for (int ww = 0; ww < NW; ++ww) M = fmaxf(M, lds[L_ML + 2 * ww]);
    float L = 0.f, o = 0.f;
    const int dd = 24 * w + min(lane, 23);
#pragma unroll
    for (int ww = 0; ww < NW; ++ww) {
      const float e = __builtin_amdgcn_exp2f(lds[L_ML + 2 * ww] - M);
      L = fmaf(e, lds[L_ML + 2 * ww + 1], L);
      o = fmaf(e, lds[L_O + ww * 192 + dd], o);
    }
    o *= 1.f / L;
    if (tid == 0) *(GAS f32x2*)(ws + WS_ML + slot * 2) = f32x2{M, L};
    const float o4 = __shfl(o, (lane + 4) & 63);
    if (lane < 24 && (lane & 4) == 0) ((LAS unsigned*)lds)[L_OP + w * 16 + (lane >> 3) * 4 + (lane & 3)] = pk2(o, o4);
    __builtin_amdgcn_fence(__ATOMIC_RELEASE, "wavefront");
    __builtin_amdgcn_wave_barrier();
    __builtin_amdgcn_fence(__ATOMIC_ACQUIRE, "wavefront");
    float ya[5][8];
#pragma unroll
    for (int k = 0; k < 5; ++k) {
      const int rr = (64 * k + lane) / 80;
#pragma unroll
      for (int e = 0; e < 8; ++e) ya[k][e] = 0.f;
#pragma unroll
      for (int p = 0; p < 3; ++p) {
        const unsigned op = ((LAS unsigned*)lds)[L_OP + w * 16 + p * 4 + rr];
        const u32x4 r0 = wor[10 * p + k], r1 = wor[10 * p + 5 + k];
#pragma unroll
        for (int e = 0; e < 4; ++e) {
          ya[k][2 * e] = dot2(__builtin_amdgcn_perm(r1[e], r0[e], 0x05040100u), op, ya[k][2 * e]);
          ya[k][2 * e + 1] = dot2(__builtin_amdgcn_perm(r1[e], r0[e], 0x07060302u), op, ya[k][2 * e + 1]);
        }
      }
    }
#pragma unroll
    for (int k = 0; k < 5; ++k) {
      LAS f32x4* p = (LAS f32x4*)((LAS float*)lds + L_RED + w * 2560 + (64 * k + lane) * 8);
      p[0] = f32x4{ya[k][0], ya[k][1], ya[k][2], ya[k][3]};
      p[1] = f32x4{ya[k][4], ya[k][5], ya[k][6], ya[k][7]};
    }
  }
  __syncthreads();
  if (tid < 320) {
    f32x2 t = {0.f, 0.f};
#pragma unroll
    for (int r = 0; r < 32; ++r) t += *(LAS f32x2*)((LAS float*)lds + L_RED + r * 640 + tid * 2);
    *(GAS f32x2*)(ws + WS_Y + slot * 640 + tid * 2) = t;
  }
}

__global__ void __launch_bounds__(64)
k2_mix(const unsigned short* __restrict__ x, const unsigned short* __restrict__ gw, const float* __restrict__ ws,
       unsigned short* __restrict__ out) {
  const int lane = threadIdx.x, b = blockIdx.x / 40, c0 = (blockIdx.x % 40) * 16;
  const int pc = lane & 3, v0 = lane >> 2;
  f32x4 y[4], f[4], ml[4];
#pragma unroll
  for (int i = 0; i < 4; ++i) {
    const int v = b * 64 + v0 + 16 * i;
    y[i] = *(const GAS f32x4*)(ws + WS_Y + v * 640 + c0 + pc * 4);
    f[i] = *(const GAS f32x4*)(ws + WS_F + v * 640 + c0 + pc * 4);
    ml[i] = *(const GAS f32x4*)(ws + WS_ML + (v & ~1) * 2);
  }
  const u32x4 xa = *(const GAS u32x4*)(x + b * D + lane * 8), ga = *(const GAS u32x4*)(gw + lane * 8);
  const int l2 = 64 + (lane & 15);
  const u32x4 xb = *(const GAS u32x4*)(x + b * D + l2 * 8), gb = *(const GAS u32x4*)(gw + l2 * 8);
  float gs = 0.f, gs2 = 0.f;
#pragma unroll
  for (int e = 0; e < 4; ++e) {
    gs += rbf(bflo(xa[e]) * bflo(ga[e])) + rbf(bfhi(xa[e]) * bfhi(ga[e]));
    gs2 += rbf(bflo(xb[e]) * bflo(gb[e])) + rbf(bfhi(xb[e]) * bfhi(gb[e]));
  }
  gs += lane < 16 ? gs2 : 0.f;
  gs += dpp<XOR1>(gs); gs += dpp<XOR2>(gs); gs += dpp<HMIR>(gs);
  gs = xsum_8_16_32(gs);
  const float g = rbf(1.f / (1.f + __expf(-rbf(gs))));
  f32x4 a = {0.f, 0.f, 0.f, 0.f}, bb = {0.f, 0.f, 0.f, 0.f};
#pragma unroll
  for (int i = 0; i < 4; ++i) {
    const float M = fmaxf(ml[i][0], ml[i][2]);
    const float w0 = __builtin_amdgcn_exp2f(ml[i][0] - M) * ml[i][1], w1 = __builtin_amdgcn_exp2f(ml[i][2] - M) * ml[i][3];
    const float wt = ((v0 & 1) ? w1 : w0) / (w0 + w1);
    a += wt * y[i];
    bb += f[i];
  }
  float r[8] = {a[0], a[1], a[2], a[3], bb[0], bb[1], bb[2], bb[3]};
#pragma unroll
  for (int k = 0; k < 8; ++k) {
    float t = r[k];
    t += __shfl_xor(t, 4);
    r[k] = xsum_8_16_32(t);
  }
  if (lane < 4) {
    float o[4];
#pragma unroll
    for (int k = 0; k < 4; ++k) o[k] = rbf(g * rbf(r[k])) + rbf(rbf(1.f - g) * rbf(r[4 + k]));
    *(GAS u32x2*)(out + b * D + c0 + pc * 4) = u32x2{pk2(o[0], o[1]), pk2(o[2], o[3])};
  }
}
}

extern "C" void kernel_launch(void* const* d_in, const int* in_sizes, int n_in, void* d_out, int out_size, void* d_ws,
                              size_t ws_size, hipStream_t stream) {
  const unsigned short* x = (const unsigned short*)d_in[0];
  const unsigned short* kc = (const unsigned short*)d_in[1];
  const unsigned short* vc = (const unsigned short*)d_in[2];
  const unsigned short* wq = (const unsigned short*)d_in[3];
  const unsigned short* wo = (const unsigned short*)d_in[4];
  const unsigned short* wg = (const unsigned short*)d_in[6];
  const unsigned short* wu = (const unsigned short*)d_in[7];
  const unsigned short* wd = (const unsigned short*)d_in[8];
  const unsigned short* gw = (const unsigned short*)d_in[9];
  float* ws = (float*)d_ws;
  hipLaunchKernelGGL(k1_attn, dim3(256), dim3(NT), 0, stream, x, kc, vc, wq, wo, wg, wu, wd, ws);
  hipLaunchKernelGGL(k2_mix, dim3(160), dim3(64), 0, stream, x, gw, (const float*)ws, (unsigned short*)d_out);
}
```

```cpp
#include <hip/hip_runtime.h>
#include <hip/hip_bf16.h>
#include <stdint.h>
#include <stdio.h>

#define GAS __attribute__((address_space(1)))
#define LAS __attribute__((address_space(3)))
typedef unsigned u32x4 __attribute__((ext_vector_type(4)));
typedef unsigned u32x2 __attribute__((ext_vector_type(2)));
typedef float f32x4 __attribute__((ext_vector_type(4)));
typedef float f32x2 __attribute__((ext_vector_type(2)));
typedef __bf16 bf16x2 __attribute__((ext_vector_type(2)));

#define RING 8
#define AUXQ 2
#define AUXW 2
#define ROTM 37

#define DELTA 12
#define BAR2 64
#define ABL_Q 0
#define ABL_O 0
#define ABL_F 0
namespace {
constexpr int H = 32, SK = 8192, DH = 192, D = 640, DF = 1536;
constexpr int NT = 512;
constexpr int ROWB = DH * 2;
constexpr int GRP_B = 8 * ROWB;
constexpr int NTILE = SK / 2 / 8;
constexpr int PER = NTILE / 4;
constexpr int R = RING;
constexpr int WS_Y = 0;
constexpr int WS_F = 256 * 640;
constexpr int WS_ML = 2 * 256 * 640;

__device__ __forceinline__ float dot2(unsigned a, unsigned b, float acc) {
  return __builtin_amdgcn_fdot2_f32_bf16(__builtin_bit_cast(bf16x2, a), __builtin_bit_cast(bf16x2, b), acc, false);
}
template <int CTRL> __device__ __forceinline__ float dpp(float x) {
  return __builtin_bit_cast(float, __builtin_amdgcn_mov_dpp(__builtin_bit_cast(int, x), CTRL, 0xf, 0xf, true));
}
constexpr int XOR1 = 0xB1, XOR2 = 0x4E, HMIR = 0x141, ROR8 = 0x128;
__device__ __forceinline__ float bflo(unsigned u) { return __uint_as_float(u << 16); }
__device__ __forceinline__ float bfhi(unsigned u) { return __uint_as_float(u & 0xffff0000u); }
__device__ __forceinline__ float bf2f(unsigned short u) { return __uint_as_float((unsigned)u << 16); }
__device__ __forceinline__ unsigned short f2bf(float f) { return __builtin_bit_cast(unsigned short, (__bf16)f); }
__device__ __forceinline__ float rbf(float f) { return bf2f(f2bf(f)); }
__device__ __forceinline__ unsigned pk2(float lo, float hi) { return (unsigned)f2bf(lo) | ((unsigned)f2bf(hi) << 16); }
__device__ __forceinline__ unsigned plo(unsigned r0, unsigned r1) { return __builtin_amdgcn_perm(r1, r0, 0x05040100u); }
__device__ __forceinline__ unsigned phi(unsigned r0, unsigned r1) { return __builtin_amdgcn_perm(r1, r0, 0x07060302u); }
__device__ __forceinline__ float swap32_add(float a, float b) {
  auto r = __builtin_amdgcn_permlane32_swap(__float_as_uint(a), __float_as_uint(b), false, false);
  return __uint_as_float(r[0]) + __uint_as_float(r[1]);
}
__device__ __forceinline__ float swap16_add(float a, float b) {
  auto r = __builtin_amdgcn_permlane16_swap(__float_as_uint(a), __float_as_uint(b), false, false);
  return __uint_as_float(r[0]) + __uint_as_float(r[1]);
}
__device__ __forceinline__ float xmax_8_16_32(float x) {
  x = fmaxf(x, dpp<ROR8>(x));
  auto s = __builtin_amdgcn_permlane16_swap(__float_as_uint(x), __float_as_uint(x), false, false);
  x = fmaxf(__uint_as_float(s[0]), __uint_as_float(s[1]));
  auto t = __builtin_amdgcn_permlane32_swap(__float_as_uint(x), __float_as_uint(x), false, false);
  return fmaxf(__uint_as_float(t[0]), __uint_as_float(t[1]));
}
__device__ __forceinline__ float xsum_8_16_32(float x) {
  x = x + dpp<ROR8>(x);
  x = swap16_add(x, x);
  return swap32_add(x, x);
}
template <int N> __device__ __forceinline__ void slot_reduce(float (&v)[N], int lane) {
#pragma unroll
  for (int j = 0; j < N / 2; ++j) v[j] = swap32_add(v[j], v[j + N / 2]);
#pragma unroll
  for (int j = 0; j < N / 4; ++j) v[j] = swap16_add(v[j], v[j + N / 4]);
#pragma unroll
  for (int j = 0; j < N / 8; ++j) {
    const float a = v[j] + dpp<ROR8>(v[j]), b = v[j + N / 8] + dpp<ROR8>(v[j + N / 8]);
    v[j] = (lane & 8) ? b : a;
  }
}
template <int AUX> __device__ __forceinline__ u32x4 bload(__amdgpu_buffer_rsrc_t r, int voff, int soff = 0) {
  return __builtin_amdgcn_raw_buffer_load_b128(r, voff, soff, AUX);
}
__device__ __forceinline__ __amdgpu_buffer_rsrc_t mkrsrc(const void* p, unsigned bytes) {
  return __builtin_amdgcn_make_buffer_rsrc((void*)p, 0, (int)bytes, 0x00020000);
}
#define SB() __builtin_amdgcn_sched_barrier(0)
#define PIN8(a, o) asm volatile("" : "+v"(a[o + 0]), "+v"(a[o + 1]), "+v"(a[o + 2]), "+v"(a[o + 3]), "+v"(a[o + 4]), "+v"(a[o + 5]), "+v"(a[o + 6]), "+v"(a[o + 7]) :: "memory")
#define PIN24(a) asm volatile("" : "+v"(a[0]), "+v"(a[1]), "+v"(a[2]), "+v"(a[3]), "+v"(a[4]), "+v"(a[5]), "+v"(a[6]), "+v"(a[7]), \
  "+v"(a[8]), "+v"(a[9]), "+v"(a[10]), "+v"(a[11]), "+v"(a[12]), "+v"(a[13]), "+v"(a[14]), "+v"(a[15]), \
  "+v"(a[16]), "+v"(a[17]), "+v"(a[18]), "+v"(a[19]), "+v"(a[20]), "+v"(a[21]), "+v"(a[22]), "+v"(a[23]) :: "memory")
#define STAMP(i)
__device__ __forceinline__ void wg_barrier() {
  asm volatile("s_waitcnt lgkmcnt(0)\n\ts_barrier" ::: "memory");
}

constexpr int L_W = 0;
constexpr int L_Q = 4 * 15 * 2 * 256;
constexpr int L_O = L_Q + 4 * 192;
constexpr int L_ML = L_O + 4 * 192;
constexpr int L_RED = L_ML + 8;
constexpr int L_G = L_RED + 4 * 640;
constexpr int L_END = L_G + 4 * 256;

__global__ void __launch_bounds__(NT, 2)
k1_attn(const unsigned short* __restrict__ x, const unsigned short* __restrict__ kc, const unsigned short* __restrict__ vc,
        const unsigned short* __restrict__ wq, const unsigned short* __restrict__ wo, const unsigned short* __restrict__ wg,
        const unsigned short* __restrict__ wu, const unsigned short* __restrict__ wd, float* __restrict__ ws) {
  __shared__ __attribute__((aligned(16))) float lds_[L_END];
  LAS float* lds = (LAS float*)lds_;
  const int tid = threadIdx.x, lane = tid & 63, w = __builtin_amdgcn_readfirstlane(tid >> 6);
  const int rs = lane >> 3, c8 = lane & 7;
  const int bid = blockIdx.x, xg = bid & 7, j = bid >> 3;
  const int hl = j & 3, h = xg * 4 + hl, mem = j >> 2, b = mem >> 1, s = mem & 1;
  const int slot = b * 64 + h * 2 + s;
  const int fl = b * 4 + hl;
  const bool fpair = (xg & 1) == 0 && fl < 12;
  const bool ffn = fpair && s == 0;
  const int fidx = (xg >> 1) * 12 + fl;
  const int t0 = fpair ? NTILE - DELTA : NTILE;
  const int ntl = s == 0 ? t0 : 2 * NTILE - t0;
  STAMP(0);
  u32x4 wf[15][2];

  if (w >= 4) {
    const int sw = w - 4;
    const size_t kvbase = ((size_t)(b * H + h) * SK + (size_t)(s * t0 * 8)) * DH;
    const __amdgpu_buffer_rsrc_t rk = mkrsrc(kc + kvbase, ntl * GRP_B);
    const __amdgpu_buffer_rsrc_t rv = mkrsrc(vc + kvbase, ntl * GRP_B);
    const int loff = rs * ROWB + c8 * 16;
    const int per = ntl >> 2;
    int tl = ((bid * ROTM) & (NTILE - 1)) + sw;
    tl = tl >= ntl ? tl - ntl : tl;
    u32x4 kb[R][3], vb[R][3];
#pragma unroll
    for (int u = 0; u < R; ++u) {
      const int o = loff + tl * GRP_B;
      tl += 4; tl = tl >= ntl ? tl - ntl : tl;
#pragma unroll
      for (int i = 0; i < 3; ++i) { kb[u][i] = bload<2>(rk, o + i * 128); SB(); }
#pragma unroll
      for (int i = 0; i < 3; ++i) { vb[u][i] = bload<2>(rv, o + i * 128); SB(); }
    }
    STAMP(1);
    wg_barrier();
    u32x4 qf[3];
#pragma unroll
    for (int i = 0; i < 3; ++i) {
      f32x4 t0 = {0.f, 0.f, 0.f, 0.f}, t1 = {0.f, 0.f, 0.f, 0.f};
#pragma unroll
      for (int hw = 0; hw < 4; ++hw) {
        t0 += *(LAS f32x4*)(lds + L_Q + hw * 192 + i * 64 + c8 * 8);
        t1 += *(LAS f32x4*)(lds + L_Q + hw * 192 + i * 64 + c8 * 8 + 4);
      }
      qf[i] = u32x4{pk2(t0[0], t0[1]), pk2(t0[2], t0[3]), pk2(t1[0], t1[1]), pk2(t1[2], t1[3])};
    }
    STAMP(2);
    constexpr float C2 = 0.07216878364870322f * 1.4426950408889634f;
    float m = -INFINITY, l = 0.f, acc[24];
#pragma unroll
    for (int k = 0; k < 24; ++k) acc[k] = 0.f;
#define STEP(u, ISSUE) do { \
        float s0 = 0.f, s1 = 0.f; \
        _Pragma("unroll") for (int i = 0; i < 3; ++i) { \
          s0 = dot2(kb[u][i][0], qf[i][0], s0); s1 = dot2(kb[u][i][1], qf[i][1], s1); \
          s0 = dot2(kb[u][i][2], qf[i][2], s0); s1 = dot2(kb[u][i][3], qf[i][3], s1); } \
        asm volatile("" : "+v"(s0), "+v"(s1) :: "memory"); \
        SB(); \
        if (ISSUE) { _Pragma("unroll") for (int i = 0; i < 3; ++i) { kb[u][i] = bload<2>(rk, o + i * 128); SB(); } } \
        float sc = s0 + s1; \
        sc += dpp<XOR1>(sc); sc += dpp<XOR2>(sc); sc += dpp<HMIR>(sc); \
        sc *= C2; \
        const float mn = fmaxf(m, sc); \
        const float alpha = __builtin_amdgcn_exp2f(m - mn), p = __builtin_amdgcn_exp2f(sc - mn); \
        m = mn; \
        l = fmaf(l, alpha, p); \
        _Pragma("unroll") for (int k = 0; k < 24; ++k) acc[k] *= alpha; \
        PIN24(acc); \
        _Pragma("unroll") for (int i = 0; i < 3; ++i) \
          _Pragma("unroll") for (int e = 0; e < 4; ++e) { \
            acc[i * 8 + 2 * e] = fmaf(p, bflo(vb[u][i][e]), acc[i * 8 + 2 * e]); \
            acc[i * 8 + 2 * e + 1] = fmaf(p, bfhi(vb[u][i][e]), acc[i * 8 + 2 * e + 1]); } \
        PIN24(acc); \
        SB(); \
        if (ISSUE) { _Pragma("unroll") for (int i = 0; i < 3; ++i) { vb[u][i] = bload<2>(rv, o + i * 128); SB(); } } \
      } while (0)
    int n = 0;
    for (; n + R <= per; n += R) {
      if (n == (BAR2 / R) * R) wg_barrier();
#pragma unroll
      for (int u = 0; u < R; ++u) {
        const int o = loff + (n + u + R < per ? tl * GRP_B : 0x40000000);
        tl += 4; tl = tl >= ntl ? tl - ntl : tl;
        STEP(u, true);
      }
    }
#pragma unroll
    for (int u = 0; u < R - 1; ++u) {
      const int o = 0;
      if (n + u < per) STEP(u, false);
    }
#undef STEP
    STAMP(3);
#pragma unroll
    for (int pj = 0; pj < 15; ++pj) {
      wf[pj][0] = *(LAS u32x4*)(lds + L_W + ((sw * 15 + pj) * 2 + 0) * 256 + lane * 4);
      wf[pj][1] = *(LAS u32x4*)(lds + L_W + ((sw * 15 + pj) * 2 + 1) * 256 + lane * 4);
    }
    const float M = xmax_8_16_32(m);
    const float sc = __builtin_amdgcn_exp2f(m - M);
    const float L = xsum_8_16_32(l * sc);
#pragma unroll
    for (int k = 0; k < 24; ++k) acc[k] *= sc;
    slot_reduce<24>(acc, lane);
#pragma unroll
    for (int jj = 0; jj < 3; ++jj) { const int k = 3 * rs + jj; lds[L_O + sw * 192 + (k >> 3) * 64 + c8 * 8 + (k & 7)] = acc[jj]; }
    if (lane == 0) *(LAS f32x2*)(lds + L_ML + 2 * sw) = f32x2{M, L};
  } else {
    {
      const __amdgpu_buffer_rsrc_t rq = mkrsrc(wq, ABL_Q ? 0 : D * H * DH * 2);
      const __amdgpu_buffer_rsrc_t rx = mkrsrc(x, 4 * D * 2);
      const int row0 = 160 * w + 20 * rs;
      const int qo0 = row0 * (H * DH * 2) + h * ROWB + c8 * 16, qo1 = qo0 + H * DH * 2;
      const int gp0 = (mem * 5) >> 2;
      unsigned xp[10];
      u32x4 wr[8][6];
#pragma unroll
      for (int g = 0; g < 10; ++g) {
        int gq = g + gp0; gq = gq >= 10 ? gq - 10 : gq;
        xp[g] = __builtin_amdgcn_raw_buffer_load_b32(rx, (b * D + row0) * 2, gq * 4, 0);
      }
      SB();
#pragma unroll
      for (int g = 0; g < 8; ++g) {
        int gq = g + gp0; gq = gq >= 10 ? gq - 10 : gq;
#pragma unroll
        for (int i = 0; i < 3; ++i) { wr[g][i] = bload<AUXQ>(rq, qo0 + i * 128, gq * (2 * H * DH * 2)); SB(); }
#pragma unroll
        for (int i = 0; i < 3; ++i) { wr[g][3 + i] = bload<AUXQ>(rq, qo1 + i * 128, gq * (2 * H * DH * 2)); SB(); }
      }
      float a[24];
#pragma unroll
      for (int k = 0; k < 24; ++k) a[k] = 0.f;
#pragma unroll
      for (int g = 0; g < 10; ++g) {
#pragma unroll
        for (int i = 0; i < 3; ++i)
#pragma unroll
          for (int e = 0; e < 4; ++e) {
            a[i * 8 + 2 * e] = dot2(plo(wr[g & 7][i][e], wr[g & 7][3 + i][e]), xp[g], a[i * 8 + 2 * e]);
            a[i * 8 + 2 * e + 1] = dot2(phi(wr[g & 7][i][e], wr[g & 7][3 + i][e]), xp[g], a[i * 8 + 2 * e + 1]);
          }
        PIN24(a);
        SB();
        if (g < 2) {
          int gq = g + 8 + gp0; gq = gq >= 10 ? gq - 10 : gq;
#pragma unroll
          for (int i = 0; i < 3; ++i) { wr[g][i] = bload<AUXQ>(rq, qo0 + i * 128, gq * (2 * H * DH * 2)); SB(); }
#pragma unroll
          for (int i = 0; i < 3; ++i) { wr[g][3 + i] = bload<AUXQ>(rq, qo1 + i * 128, gq * (2 * H * DH * 2)); SB(); }
        }
      }
      slot_reduce<24>(a, lane);
#pragma unroll
      for (int jj = 0; jj < 3; ++jj) { const int k = 3 * rs + jj; lds[L_Q + w * 192 + (k >> 3) * 64 + c8 * 8 + (k & 7)] = a[jj]; }
    }
    STAMP(1);
    wg_barrier();
    const __amdgpu_buffer_rsrc_t ro = mkrsrc(wo + (size_t)h * DH * D, ABL_O ? 0 : DH * D * 2);
    {
      const int tw = 4 + w;
      const int obase = (48 * (tw >> 1) + rs) * (D * 2) + (tw & 1) * 640 + c8 * 16;
#pragma unroll 1
      for (int jl = 0; jl < 5; ++jl) {
        u32x4 t[3][2];
#pragma unroll
        for (int p = 0; p < 3; ++p) {
          t[p][0] = bload<AUXW>(ro, obase + jl * 128, (16 * p) * (D * 2));
          t[p][1] = bload<AUXW>(ro, obase + jl * 128, (16 * p + 8) * (D * 2));
        }
#pragma unroll
        for (int p = 0; p < 3; ++p) {
          LAS u32x4* dst = (LAS u32x4*)(lds + L_W + ((w * 15 + jl * 3 + p) * 2) * 256 + lane * 4);
          dst[0] = u32x4{plo(t[p][0][0], t[p][1][0]), phi(t[p][0][0], t[p][1][0]), plo(t[p][0][1], t[p][1][1]), phi(t[p][0][1], t[p][1][1])};
          dst[64] = u32x4{plo(t[p][0][2], t[p][1][2]), phi(t[p][0][2], t[p][1][2]), plo(t[p][0][3], t[p][1][3]), phi(t[p][0][3], t[p][1][3])};
        }
      }
    }
    STAMP(2);
    {
      const int obase = (48 * (w >> 1) + rs) * (D * 2) + (w & 1) * 640 + c8 * 16;
#pragma unroll
      for (int jl = 0; jl < 5; ++jl) {
        u32x4 t[3][2];
#pragma unroll
        for (int p = 0; p < 3; ++p) {
          t[p][0] = bload<AUXW>(ro, obase + jl * 128, (16 * p) * (D * 2));
          t[p][1] = bload<AUXW>(ro, obase + jl * 128, (16 * p + 8) * (D * 2));
        }
#pragma unroll
        for (int p = 0; p < 3; ++p) {
          wf[jl * 3 + p][0] = u32x4{plo(t[p][0][0], t[p][1][0]), phi(t[p][0][0], t[p][1][0]), plo(t[p][0][1], t[p][1][1]), phi(t[p][0][1], t[p][1][1])};
          wf[jl * 3 + p][1] = u32x4{plo(t[p][0][2], t[p][1][2]), phi(t[p][0][2], t[p][1][2]), plo(t[p][0][3], t[p][1][3]), phi(t[p][0][3], t[p][1][3])};
          asm volatile("" : "+v"(wf[jl * 3 + p][0]), "+v"(wf[jl * 3 + p][1]) :: "memory");
        }
        SB();
      }
    }
    if (ffn && !ABL_F) {
      const int row0 = 160 * w + 20 * rs;
      const GAS unsigned short* gp = (const GAS unsigned short*)(c8 < 4 ? wg : wu) + (size_t)row0 * DF + fidx * 32 + (c8 & 3) * 8;
      const GAS unsigned* xq = (const GAS unsigned*)(x + row0);
      float ga[32];
#pragma unroll
      for (int k = 0; k < 32; ++k) ga[k] = 0.f;
#pragma unroll 1
      for (int g = 0; g < 5; ++g) {
        u32x4 t[4];
        u32x2 xx[4];
#pragma unroll
        for (int r = 0; r < 4; ++r) t[r] = __builtin_nontemporal_load((const GAS u32x4*)(gp + (size_t)(g * 4 + r) * DF));
#pragma unroll
        for (int bb = 0; bb < 4; ++bb) xx[bb] = *(const GAS u32x2*)(xq + bb * (D / 2) + g * 2);
#pragma unroll
        for (int r = 0; r < 2; ++r)
#pragma unroll
          for (int e = 0; e < 4; ++e) {
            const unsigned lo = plo(t[2 * r][e], t[2 * r + 1][e]), hi = phi(t[2 * r][e], t[2 * r + 1][e]);
#pragma unroll
            for (int bb = 0; bb < 4; ++bb) {
              ga[bb * 8 + 2 * e] = dot2(lo, xx[bb][r], ga[bb * 8 + 2 * e]);
              ga[bb * 8 + 2 * e + 1] = dot2(hi, xx[bb][r], ga[bb * 8 + 2 * e + 1]);
            }
          }
      }
      slot_reduce<32>(ga, lane);
      *(LAS f32x4*)(lds + L_G + w * 256 + (rs >> 1) * 64 + c8 * 8 + (rs & 1) * 4) = f32x4{ga[0], ga[1], ga[2], ga[3]};
    }
    STAMP(3);
    wg_barrier();
    if (ffn && !ABL_F) {
      float hr[4][4];
#pragma unroll
      for (int bb = 0; bb < 4; ++bb)
#pragma unroll
        for (int rg = 0; rg < 4; ++rg) {
          float g = 0.f, u = 0.f;
#pragma unroll
          for (int hw = 0; hw < 4; ++hw) { g += lds[L_G + hw * 256 + bb * 64 + 8 * rg + rs]; u += lds[L_G + hw * 256 + bb * 64 + 32 + 8 * rg + rs]; }
          g = rbf(g); u = rbf(u);
          const float inner = 0.7978845608028654f * (g + 0.044715f * g * g * g);
          const float th = 1.f - 2.f / (1.f + __expf(2.f * inner));
          hr[bb][rg] = rbf(rbf(0.5f * g * (1.f + th)) * u);
        }
      const __amdgpu_buffer_rsrc_t rd = mkrsrc(wd + (size_t)fidx * 32 * D, 32 * D * 2);
      const int dbase = rs * (D * 2) + c8 * 16;
#pragma unroll 1
      for (int jl = w; jl < 10; jl += 4) {
        u32x4 t[4];
#pragma unroll
        for (int rg = 0; rg < 4; ++rg) t[rg] = bload<2>(rd, dbase + jl * 128, rg * 8 * (D * 2));
        float fa[32];
#pragma unroll
        for (int bb = 0; bb < 4; ++bb)
#pragma unroll
          for (int e = 0; e < 4; ++e) {
            fa[bb * 8 + 2 * e] = hr[bb][0] * bflo(t[0][e]) + hr[bb][1] * bflo(t[1][e]) + hr[bb][2] * bflo(t[2][e]) + hr[bb][3] * bflo(t[3][e]);
            fa[bb * 8 + 2 * e + 1] = hr[bb][0] * bfhi(t[0][e]) + hr[bb][1] * bfhi(t[1][e]) + hr[bb][2] * bfhi(t[2][e]) + hr[bb][3] * bfhi(t[3][e]);
          }
        slot_reduce<32>(fa, lane);
        *(GAS f32x4*)(ws + WS_F + ((rs >> 1) * 48 + fidx) * 640 + jl * 64 + c8 * 8 + (rs & 1) * 4) = f32x4{fa[0], fa[1], fa[2], fa[3]};
      }
    }
  }
  STAMP(4);
  wg_barrier();
  STAMP(5);
  {
    const f32x4 ml0 = *(LAS f32x4*)(lds + L_ML), ml1 = *(LAS f32x4*)(lds + L_ML + 4);
    const float M = fmaxf(fmaxf(ml0[0], ml0[2]), fmaxf(ml1[0], ml1[2]));
    float e[4] = {__builtin_amdgcn_exp2f(ml0[0] - M), __builtin_amdgcn_exp2f(ml0[2] - M), __builtin_amdgcn_exp2f(ml1[0] - M), __builtin_amdgcn_exp2f(ml1[2] - M)};
    const float L = e[0] * ml0[1] + e[1] * ml0[3] + e[2] * ml1[1] + e[3] * ml1[3];
    const float inv = 1.f / L;
    if (tid == 0) *(GAS f32x2*)(ws + WS_ML + slot * 2) = f32x2{M, L};
    unsigned op[3];
    const int r0 = 48 * (w >> 1) + rs;
#pragma unroll
    for (int p = 0; p < 3; ++p) {
      float o0 = 0.f, o1 = 0.f;
#pragma unroll
      for (int sw = 0; sw < 4; ++sw) {
        o0 = fmaf(e[sw], lds[L_O + sw * 192 + r0 + 16 * p], o0);
        o1 = fmaf(e[sw], lds[L_O + sw * 192 + r0 + 16 * p + 8], o1);
      }
      op[p] = pk2(o0 * inv, o1 * inv);
    }
#pragma unroll
    for (int jl = 0; jl < 5; ++jl) {
      float ya[8];
#pragma unroll
      for (int k = 0; k < 8; ++k) {
        ya[k] = dot2(wf[jl * 3][k >> 2][k & 3], op[0], 0.f);
        ya[k] = dot2(wf[jl * 3 + 1][k >> 2][k & 3], op[1], ya[k]);
        ya[k] = dot2(wf[jl * 3 + 2][k >> 2][k & 3], op[2], ya[k]);
      }
      slot_reduce<8>(ya, lane);
      lds[L_RED + (w >> 1) * 640 + (w & 1) * 320 + jl * 64 + c8 * 8 + rs] = ya[0];
    }
  }
  wg_barrier();
  if (tid < 320) {
    f32x2 t = *(LAS f32x2*)(lds + L_RED + tid * 2);
#pragma unroll
    for (int r = 1; r < 4; ++r) t += *(LAS f32x2*)(lds + L_RED + r * 640 + tid * 2);
    *(GAS f32x2*)(ws + WS_Y + slot * 640 + tid * 2) = t;
  }
}

__global__ void __launch_bounds__(64)
k2_mix(const unsigned short* __restrict__ x, const unsigned short* __restrict__ gw, const float* __restrict__ ws,
       unsigned short* __restrict__ out) {
  const int lane = threadIdx.x, b = blockIdx.x / 40, c0 = (blockIdx.x % 40) * 16;
  const int pc = lane & 3, v0 = lane >> 2;
  f32x4 y[4], f[3], ml[4];
#pragma unroll
  for (int i = 0; i < 4; ++i) {
    const int v = b * 64 + v0 + 16 * i;
    y[i] = *(const GAS f32x4*)(ws + WS_Y + v * 640 + c0 + pc * 4);
    if (i < 3) f[i] = *(const GAS f32x4*)(ws + WS_F + (b * 48 + v0 + 16 * i) * 640 + c0 + pc * 4);
    ml[i] = *(const GAS f32x4*)(ws + WS_ML + (v & ~1) * 2);
  }
  const u32x4 xa = *(const GAS u32x4*)(x + b * D + lane * 8), ga = *(const GAS u32x4*)(gw + lane * 8);
  const int l2 = 64 + (lane & 15);
  const u32x4 xb = *(const GAS u32x4*)(x + b * D + l2 * 8), gb = *(const GAS u32x4*)(gw + l2 * 8);
  SB();
  float gs = 0.f, gs2 = 0.f;
#pragma unroll
  for (int e = 0; e < 4; ++e) {
    gs += rbf(bflo(xa[e]) * bflo(ga[e])) + rbf(bfhi(xa[e]) * bfhi(ga[e]));
    gs2 += rbf(bflo(xb[e]) * bflo(gb[e])) + rbf(bfhi(xb[e]) * bfhi(gb[e]));
  }
  gs += lane < 16 ? gs2 : 0.f;
  gs += dpp<XOR1>(gs); gs += dpp<XOR2>(gs); gs += dpp<HMIR>(gs);
  gs = xsum_8_16_32(gs);
  const float g = rbf(1.f / (1.f + __expf(-rbf(gs))));
  f32x4 a = {0.f, 0.f, 0.f, 0.f}, bb = {0.f, 0.f, 0.f, 0.f};
#pragma unroll
  for (int i = 0; i < 4; ++i) {
    const float M = fmaxf(ml[i][0], ml[i][2]);
    const float w0 = __builtin_amdgcn_exp2f(ml[i][0] - M) * ml[i][1], w1 = __builtin_amdgcn_exp2f(ml[i][2] - M) * ml[i][3];
    const float wt = ((v0 & 1) ? w1 : w0) / (w0 + w1);
    a += wt * y[i];
    if (i < 3) bb += f[i];
  }
  float r[8] = {a[0], a[1], a[2], a[3], bb[0], bb[1], bb[2], bb[3]};
#pragma unroll
  for (int k = 0; k < 8; ++k) {
    float t = r[k];
    t += __shfl_xor(t, 4);
    r[k] = xsum_8_16_32(t);
  }
  if (lane < 4) {
    float o[4];
#pragma unroll
    for (int k = 0; k < 4; ++k) o[k] = rbf(g * rbf(r[k])) + rbf(rbf(1.f - g) * rbf(r[4 + k]));
    *(GAS u32x2*)(out + b * D + c0 + pc * 4) = u32x2{pk2(o[0], o[1]), pk2(o[2], o[3])};
  }
}
}

extern "C" void kernel_launch(void* const* d_in, const int* in_sizes, int n_in, void* d_out, int out_size, void* d_ws,
                              size_t ws_size, hipStream_t stream) {
  const unsigned short* x = (const unsigned short*)d_in[0];
  const unsigned short* kc = (const unsigned short*)d_in[1];
  const unsigned short* vc = (const unsigned short*)d_in[2];
  const unsigned short* wq = (const unsigned short*)d_in[3];
  const unsigned short* wo = (const unsigned short*)d_in[4];
  const unsigned short* wg = (const unsigned short*)d_in[6];
  const unsigned short* wu = (const unsigned short*)d_in[7];
  const unsigned short* wd = (const unsigned short*)d_in[8];
  const unsigned short* gw = (const unsigned short*)d_in[9];
  float* ws = (float*)d_ws;
  for (int i = 0; i < 10; ++i) { hipPointerAttribute_t at; hipError_t e = hipPointerGetAttributes(&at, d_in[i]); fprintf(stderr, "in[%d] %p n=%d err=%d type=%d managed=%d flags=%u\n", i, d_in[i], in_sizes[i], (int)e, (int)at.type, (int)at.isManaged, at.allocationFlags); }
  fprintf(stderr, "out %p ws %p\n", d_out, d_ws);
  hipLaunchKernelGGL(k1_attn, dim3(256), dim3(NT), 0, stream, x, kc, vc, wq, wo, wg, wu, wd, ws);
  hipLaunchKernelGGL(k2_mix, dim3(160), dim3(64), 0, stream, x, gw, (const float*)ws, (unsigned short*)d_out);
}
```

```cpp
#include <hip/hip_runtime.h>
#include <hip/hip_bf16.h>
#include <stdint.h>

#define GAS __attribute__((address_space(1)))
#define LAS __attribute__((address_space(3)))
typedef unsigned u32x4 __attribute__((ext_vector_type(4)));
typedef unsigned u32x2 __attribute__((ext_vector_type(2)));
typedef float f32x4 __attribute__((ext_vector_type(4)));
typedef float f32x2 __attribute__((ext_vector_type(2)));
typedef __bf16 bf16x2 __attribute__((ext_vector_type(2)));

#define RING 8
#define AUXQ 0
#define AUXW 0
#define ROTM 37

#define DELTA 12
#define BAR2 64
#define ABL_Q 0
#define ABL_O 0
#define ABL_F 0
namespace {
constexpr int H = 32, SK = 8192, DH = 192, D = 640, DF = 1536;
constexpr int NT = 512;
constexpr int ROWB = DH * 2;
constexpr int GRP_B = 8 * ROWB;
constexpr int NTILE = SK / 2 / 8;
constexpr int PER = NTILE / 4;
constexpr int R = RING;
constexpr int WS_Y = 0;
constexpr int WS_F = 256 * 640;
constexpr int WS_ML = 2 * 256 * 640;

__device__ __forceinline__ float dot2(unsigned a, unsigned b, float acc) {
  return __builtin_amdgcn_fdot2_f32_bf16(__builtin_bit_cast(bf16x2, a), __builtin_bit_cast(bf16x2, b), acc, false);
}
template <int CTRL> __device__ __forceinline__ float dpp(float x) {
  return __builtin_bit_cast(float, __builtin_amdgcn_mov_dpp(__builtin_bit_cast(int, x), CTRL, 0xf, 0xf, true));
}
constexpr int XOR1 = 0xB1, XOR2 = 0x4E, HMIR = 0x141, ROR8 = 0x128;
__device__ __forceinline__ float bflo(unsigned u) { return __uint_as_float(u << 16); }
__device__ __forceinline__ float bfhi(unsigned u) { return __uint_as_float(u & 0xffff0000u); }
__device__ __forceinline__ float bf2f(unsigned short u) { return __uint_as_float((unsigned)u << 16); }
__device__ __forceinline__ unsigned short f2bf(float f) { return __builtin_bit_cast(unsigned short, (__bf16)f); }
__device__ __forceinline__ float rbf(float f) { return bf2f(f2bf(f)); }
__device__ __forceinline__ unsigned pk2(float lo, float hi) { return (unsigned)f2bf(lo) | ((unsigned)f2bf(hi) << 16); }
__device__ __forceinline__ unsigned plo(unsigned r0, unsigned r1) { return __builtin_amdgcn_perm(r1, r0, 0x05040100u); }
__device__ __forceinline__ unsigned phi(unsigned r0, unsigned r1) { return __builtin_amdgcn_perm(r1, r0, 0x07060302u); }
__device__ __forceinline__ float swap32_add(float a, float b) {
  auto r = __builtin_amdgcn_permlane32_swap(__float_as_uint(a), __float_as_uint(b), false, false);
  return __uint_as_float(r[0]) + __uint_as_float(r[1]);
}
__device__ __forceinline__ float swap16_add(float a, float b) {
  auto r = __builtin_amdgcn_permlane16_swap(__float_as_uint(a), __float_as_uint(b), false, false);
  return __uint_as_float(r[0]) + __uint_as_float(r[1]);
}
__device__ __forceinline__ float xmax_8_16_32(float x) {
  x = fmaxf(x, dpp<ROR8>(x));
  auto s = __builtin_amdgcn_permlane16_swap(__float_as_uint(x), __float_as_uint(x), false, false);
  x = fmaxf(__uint_as_float(s[0]), __uint_as_float(s[1]));
  auto t = __builtin_amdgcn_permlane32_swap(__float_as_uint(x), __float_as_uint(x), false, false);
  return fmaxf(__uint_as_float(t[0]), __uint_as_float(t[1]));
}
__device__ __forceinline__ float xsum_8_16_32(float x) {
  x = x + dpp<ROR8>(x);
  x = swap16_add(x, x);
  return swap32_add(x, x);
}
template <int N> __device__ __forceinline__ void slot_reduce(float (&v)[N], int lane) {
#pragma unroll
  for (int j = 0; j < N / 2; ++j) v[j] = swap32_add(v[j], v[j + N / 2]);
#pragma unroll
  for (int j = 0; j < N / 4; ++j) v[j] = swap16_add(v[j], v[j + N / 4]);
#pragma unroll
  for (int j = 0; j < N / 8; ++j) {
    const float a = v[j] + dpp<ROR8>(v[j]), b = v[j + N / 8] + dpp<ROR8>(v[j + N / 8]);
    v[j] = (lane & 8) ? b : a;
  }
}
template <int AUX> __device__ __forceinline__ u32x4 bload(__amdgpu_buffer_rsrc_t r, int voff, int soff = 0) {
  return __builtin_amdgcn_raw_buffer_load_b128(r, voff, soff, AUX);
}
__device__ __forceinline__ __amdgpu_buffer_rsrc_t mkrsrc(const void* p, unsigned bytes) {
  return __builtin_amdgcn_make_buffer_rsrc((void*)p, 0, (int)bytes, 0x00020000);
}
#define SB() __builtin_amdgcn_sched_barrier(0)
#define PIN8(a, o) asm volatile("" : "+v"(a[o + 0]), "+v"(a[o + 1]), "+v"(a[o + 2]), "+v"(a[o + 3]), "+v"(a[o + 4]), "+v"(a[o + 5]), "+v"(a[o + 6]), "+v"(a[o + 7]) :: "memory")
#define PIN24(a) asm volatile("" : "+v"(a[0]), "+v"(a[1]), "+v"(a[2]), "+v"(a[3]), "+v"(a[4]), "+v"(a[5]), "+v"(a[6]), "+v"(a[7]), \
  "+v"(a[8]), "+v"(a[9]), "+v"(a[10]), "+v"(a[11]), "+v"(a[12]), "+v"(a[13]), "+v"(a[14]), "+v"(a[15]), \
  "+v"(a[16]), "+v"(a[17]), "+v"(a[18]), "+v"(a[19]), "+v"(a[20]), "+v"(a[21]), "+v"(a[22]), "+v"(a[23]) :: "memory")
#define STAMP(i)
__device__ __forceinline__ void wg_barrier() {
  asm volatile("s_waitcnt lgkmcnt(0)\n\ts_barrier" ::: "memory");
}

constexpr int L_W = 0;
constexpr int L_Q = 4 * 15 * 2 * 256;
constexpr int L_O = L_Q + 4 * 192;
constexpr int L_ML = L_O + 4 * 192;
constexpr int L_RED = L_ML + 8;
constexpr int L_G = L_RED + 4 * 640;
constexpr int L_END = L_G + 4 * 256;

__global__ void __launch_bounds__(NT, 2)
k1_attn(const unsigned short* __restrict__ x, const unsigned short* __restrict__ kc, const unsigned short* __restrict__ vc,
        const unsigned short* __restrict__ wq, const unsigned short* __restrict__ wo, const unsigned short* __restrict__ wg,
        const unsigned short* __restrict__ wu, const unsigned short* __restrict__ wd, float* __restrict__ ws) {
  __shared__ __attribute__((aligned(16))) float lds_[L_END];
  LAS float* lds = (LAS float*)lds_;
  const int tid = threadIdx.x, lane = tid & 63, w = __builtin_amdgcn_readfirstlane(tid >> 6);
  const int rs = lane >> 3, c8 = lane & 7;
  const int bid = blockIdx.x, xg = bid & 7, j = bid >> 3;
  const int hl = j & 3, h = xg * 4 + hl, mem = j >> 2, b = mem >> 1, s = mem & 1;
  const int slot = b * 64 + h * 2 + s;
  const int fl = b * 4 + hl;
  const bool fpair = (xg & 1) == 0 && fl < 12;
  const bool ffn = fpair && s == 0;
  const int fidx = (xg >> 1) * 12 + fl;
  const int t0 = fpair ? NTILE - DELTA : NTILE;
  const int ntl = s == 0 ? t0 : 2 * NTILE - t0;
  STAMP(0);
  u32x4 wf[15][2];

  if (w >= 4) {
    const int sw = w - 4;
    const size_t kvbase = ((size_t)(b * H + h) * SK + (size_t)(s * t0 * 8)) * DH;
    const __amdgpu_buffer_rsrc_t rk = mkrsrc(kc + kvbase, ntl * GRP_B);
    const __amdgpu_buffer_rsrc_t rv = mkrsrc(vc + kvbase, ntl * GRP_B);
    const int loff = rs * ROWB + c8 * 16;
    const int per = ntl >> 2;
    int tl = ((bid * ROTM) & (NTILE - 1)) + sw;
    tl = tl >= ntl ? tl - ntl : tl;
    u32x4 kb[R][3], vb[R][3];
#pragma unroll
    for (int u = 0; u < R; ++u) {
      const int o = loff + tl * GRP_B;
      tl += 4; tl = tl >= ntl ? tl - ntl : tl;
#pragma unroll
      for (int i = 0; i < 3; ++i) { kb[u][i] = bload<2>(rk, o + i * 128); SB(); }
#pragma unroll
      for (int i = 0; i < 3; ++i) { vb[u][i] = bload<2>(rv, o + i * 128); SB(); }
    }
    STAMP(1);
    wg_barrier();
    u32x4 qf[3];
#pragma unroll
    for (int i = 0; i < 3; ++i) {
      f32x4 t0 = {0.f, 0.f, 0.f, 0.f}, t1 = {0.f, 0.f, 0.f, 0.f};
#pragma unroll
      for (int hw = 0; hw < 4; ++hw) {
        t0 += *(LAS f32x4*)(lds + L_Q + hw * 192 + i * 64 + c8 * 8);
        t1 += *(LAS f32x4*)(lds + L_Q + hw * 192 + i * 64 + c8 * 8 + 4);
      }
      qf[i] = u32x4{pk2(t0[0], t0[1]), pk2(t0[2], t0[3]), pk2(t1[0], t1[1]), pk2(t1[2], t1[3])};
    }
    STAMP(2);
    constexpr float C2 = 0.07216878364870322f * 1.4426950408889634f;
    float m = -INFINITY, l = 0.f, acc[24];
#pragma unroll
    for (int k = 0; k < 24; ++k) acc[k] = 0.f;
#define STEP(u, ISSUE) do { \
        float s0 = 0.f, s1 = 0.f; \
        _Pragma("unroll") for (int i = 0; i < 3; ++i) { \
          s0 = dot2(kb[u][i][0], qf[i][0], s0); s1 = dot2(kb[u][i][1], qf[i][1], s1); \
          s0 = dot2(kb[u][i][2], qf[i][2], s0); s1 = dot2(kb[u][i][3], qf[i][3], s1); } \
        asm volatile("" : "+v"(s0), "+v"(s1) :: "memory"); \
        SB(); \
        if (ISSUE) { _Pragma("unroll") for (int i = 0; i < 3; ++i) { kb[u][i] = bload<2>(rk, o + i * 128); SB(); } } \
        float sc = s0 + s1; \
        sc += dpp<XOR1>(sc); sc += dpp<XOR2>(sc); sc += dpp<HMIR>(sc); \
        sc *= C2; \
        const float mn = fmaxf(m, sc); \
        const float alpha = __builtin_amdgcn_exp2f(m - mn), p = __builtin_amdgcn_exp2f(sc - mn); \
        m = mn; \
        l = fmaf(l, alpha, p); \
        _Pragma("unroll") for (int k = 0; k < 24; ++k) acc[k] *= alpha; \
        PIN24(acc); \
        _Pragma("unroll") for (int i = 0; i < 3; ++i) \
          _Pragma("unroll") for (int e = 0; e < 4; ++e) { \
            acc[i * 8 + 2 * e] = fmaf(p, bflo(vb[u][i][e]), acc[i * 8 + 2 * e]); \
            acc[i * 8 + 2 * e + 1] = fmaf(p, bfhi(vb[u][i][e]), acc[i * 8 + 2 * e + 1]); } \
        PIN24(acc); \
        SB(); \
        if (ISSUE) { _Pragma("unroll") for (int i = 0; i < 3; ++i) { vb[u][i] = bload<2>(rv, o + i * 128); SB(); } } \
      } while (0)
    int n = 0;
    for (; n + R <= per; n += R) {
      if (n == (BAR2 / R) * R) wg_barrier();
#pragma unroll
      for (int u = 0; u < R; ++u) {
        const int o = loff + (n + u + R < per ? tl * GRP_B : 0x40000000);
        tl += 4; tl = tl >= ntl ? tl - ntl : tl;
        STEP(u, true);
      }
    }
#pragma unroll
    for (int u = 0; u < R - 1; ++u) {
      const int o = 0;
      if (n + u < per) STEP(u, false);
    }
#undef STEP
    STAMP(3);
#pragma unroll
    for (int pj = 0; pj < 15; ++pj) {
      wf[pj][0] = *(LAS u32x4*)(lds + L_W + ((sw * 15 + pj) * 2 + 0) * 256 + lane * 4);
      wf[pj][1] = *(LAS u32x4*)(lds + L_W + ((sw * 15 + pj) * 2 + 1) * 256 + lane * 4);
    }
    const float M = xmax_8_16_32(m);
    const float sc = __builtin_amdgcn_exp2f(m - M);
    const float L = xsum_8_16_32(l * sc);
#pragma unroll
    for (int k = 0; k < 24; ++k) acc[k] *= sc;
    slot_reduce<24>(acc, lane);
#pragma unroll
    for (int jj = 0; jj < 3; ++jj) { const int k = 3 * rs + jj; lds[L_O + sw * 192 + (k >> 3) * 64 + c8 * 8 + (k & 7)] = acc[jj]; }
    if (lane == 0) *(LAS f32x2*)(lds + L_ML + 2 * sw) = f32x2{M, L};
  } else {
    {
      const __amdgpu_buffer_rsrc_t rq = mkrsrc(wq, ABL_Q ? 0 : D * H * DH * 2);
      const __amdgpu_buffer_rsrc_t rx = mkrsrc(x, 4 * D * 2);
      const int row0 = 160 * w + 20 * rs;
      const int qo0 = row0 * (H * DH * 2) + h * ROWB + c8 * 16, qo1 = qo0 + H * DH * 2;
      const int gp0 = (mem * 5) >> 2;
      unsigned xp[10];
      u32x4 wr[8][6];
#pragma unroll
      for (int g = 0; g < 10; ++g) {
        int gq = g + gp0; gq = gq >= 10 ? gq - 10 : gq;
        xp[g] = __builtin_amdgcn_raw_buffer_load_b32(rx, (b * D + row0) * 2, gq * 4, 0);
      }
      SB();
#pragma unroll
      for (int g = 0; g < 8; ++g) {
        int gq = g + gp0; gq = gq >= 10 ? gq - 10 : gq;
#pragma unroll
        for (int i = 0; i < 3; ++i) { wr[g][i] = bload<AUXQ>(rq, qo0 + i * 128, gq * (2 * H * DH * 2)); SB(); }
#pragma unroll
        for (int i = 0; i < 3; ++i) { wr[g][3 + i] = bload<AUXQ>(rq, qo1 + i * 128, gq * (2 * H * DH * 2)); SB(); }
      }
      float a[24];
#pragma unroll
      for (int k = 0; k < 24; ++k) a[k] = 0.f;
#pragma unroll
      for (int g = 0; g < 10; ++g) {
#pragma unroll
        for (int i = 0; i < 3; ++i)
#pragma unroll
          for (int e = 0; e < 4; ++e) {
            a[i * 8 + 2 * e] = dot2(plo(wr[g & 7][i][e], wr[g & 7][3 + i][e]), xp[g], a[i * 8 + 2 * e]);
            a[i * 8 + 2 * e + 1] = dot2(phi(wr[g & 7][i][e], wr[g & 7][3 + i][e]), xp[g], a[i * 8 + 2 * e + 1]);
          }
        PIN24(a);
        SB();
        if (g < 2) {
          int gq = g + 8 + gp0; gq = gq >= 10 ? gq - 10 : gq;
#pragma unroll
          for (int i = 0; i < 3; ++i) { wr[g][i] = bload<AUXQ>(rq, qo0 + i * 128, gq * (2 * H * DH * 2)); SB(); }
#pragma unroll
          for (int i = 0; i < 3; ++i) { wr[g][3 + i] = bload<AUXQ>(rq, qo1 + i * 128, gq * (2 * H * DH * 2)); SB(); }
        }
      }
      slot_reduce<24>(a, lane);
#pragma unroll
      for (int jj = 0; jj < 3; ++jj) { const int k = 3 * rs + jj; lds[L_Q + w * 192 + (k >> 3) * 64 + c8 * 8 + (k & 7)] = a[jj]; }
    }
    STAMP(1);
    wg_barrier();
    const __amdgpu_buffer_rsrc_t ro = mkrsrc(wo + (size_t)h * DH * D, ABL_O ? 0 : DH * D * 2);
    {
      const int tw = 4 + w;
      const int obase = (48 * (tw >> 1) + rs) * (D * 2) + (tw & 1) * 640 + c8 * 16;
#pragma unroll 1
      for (int jl = 0; jl < 5; ++jl) {
        u32x4 t[3][2];
#pragma unroll
        for (int p = 0; p < 3; ++p) {
          t[p][0] = bload<AUXW>(ro, obase + jl * 128, (16 * p) * (D * 2));
          t[p][1] = bload<AUXW>(ro, obase + jl * 128, (16 * p + 8) * (D * 2));
        }
#pragma unroll
        for (int p = 0; p < 3; ++p) {
          LAS u32x4* dst = (LAS u32x4*)(lds + L_W + ((w * 15 + jl * 3 + p) * 2) * 256 + lane * 4);
          dst[0] = u32x4{plo(t[p][0][0], t[p][1][0]), phi(t[p][0][0], t[p][1][0]), plo(t[p][0][1], t[p][1][1]), phi(t[p][0][1], t[p][1][1])};
          dst[64] = u32x4{plo(t[p][0][2], t[p][1][2]), phi(t[p][0][2], t[p][1][2]), plo(t[p][0][3], t[p][1][3]), phi(t[p][0][3], t[p][1][3])};
        }
      }
    }
    STAMP(2);
    {
      const int obase = (48 * (w >> 1) + rs) * (D * 2) + (w & 1) * 640 + c8 * 16;
#pragma unroll
      for (int jl = 0; jl < 5; ++jl) {
        u32x4 t[3][2];
#pragma unroll
        for (int p = 0; p < 3; ++p) {
          t[p][0] = bload<AUXW>(ro, obase + jl * 128, (16 * p) * (D * 2));
          t[p][1] = bload<AUXW>(ro, obase + jl * 128, (16 * p + 8) * (D * 2));
        }
#pragma unroll
        for (int p = 0; p < 3; ++p) {
          wf[jl * 3 + p][0] = u32x4{plo(t[p][0][0], t[p][1][0]), phi(t[p][0][0], t[p][1][0]), plo(t[p][0][1], t[p][1][1]), phi(t[p][0][1], t[p][1][1])};
          wf[jl * 3 + p][1] = u32x4{plo(t[p][0][2], t[p][1][2]), phi(t[p][0][2], t[p][1][2]), plo(t[p][0][3], t[p][1][3]), phi(t[p][0][3], t[p][1][3])};
          asm volatile("" : "+v"(wf[jl * 3 + p][0]), "+v"(wf[jl * 3 + p][1]) :: "memory");
        }
        SB();
      }
    }
    if (ffn && !ABL_F) {
      const int row0 = 160 * w + 20 * rs;
      const GAS unsigned short* gp = (const GAS unsigned short*)(c8 < 4 ? wg : wu) + (size_t)row0 * DF + fidx * 32 + (c8 & 3) * 8;
      const GAS unsigned* xq = (const GAS unsigned*)(x + row0);
      float ga[32];
#pragma unroll
      for (int k = 0; k < 32; ++k) ga[k] = 0.f;
#pragma unroll 1
      for (int g = 0; g < 5; ++g) {
        u32x4 t[4];
        u32x2 xx[4];
#pragma unroll
        for (int r = 0; r < 4; ++r) t[r] = __builtin_nontemporal_load((const GAS u32x4*)(gp + (size_t)(g * 4 + r) * DF));
#pragma unroll
        for (int bb = 0; bb < 4; ++bb) xx[bb] = *(const GAS u32x2*)(xq + bb * (D / 2) + g * 2);
#pragma unroll
        for (int r = 0; r < 2; ++r)
#pragma unroll
          for (int e = 0; e < 4; ++e) {
            const unsigned lo = plo(t[2 * r][e], t[2 * r + 1][e]), hi = phi(t[2 * r][e], t[2 * r + 1][e]);
#pragma unroll
            for (int bb = 0; bb < 4; ++bb) {
              ga[bb * 8 + 2 * e] = dot2(lo, xx[bb][r], ga[bb * 8 + 2 * e]);
              ga[bb * 8 + 2 * e + 1] = dot2(hi, xx[bb][r], ga[bb * 8 + 2 * e + 1]);
            }
          }
      }
      slot_reduce<32>(ga, lane);
      *(LAS f32x4*)(lds + L_G + w * 256 + (rs >> 1) * 64 + c8 * 8 + (rs & 1) * 4) = f32x4{ga[0], ga[1], ga[2], ga[3]};
    }
    STAMP(3);
    wg_barrier();
    if (ffn && !ABL_F) {
      float hr[4][4];
#pragma unroll
      for (int bb = 0; bb < 4; ++bb)
#pragma unroll
        for (int rg = 0; rg < 4; ++rg) {
          float g = 0.f, u = 0.f;
#pragma unroll
          for (int hw = 0; hw < 4; ++hw) { g += lds[L_G + hw * 256 + bb * 64 + 8 * rg + rs]; u += lds[L_G + hw * 256 + bb * 64 + 32 + 8 * rg + rs]; }
          g = rbf(g); u = rbf(u);
          const float inner = 0.7978845608028654f * (g + 0.044715f * g * g * g);
          const float th = 1.f - 2.f / (1.f + __expf(2.f * inner));
          hr[bb][rg] = rbf(rbf(0.5f * g * (1.f + th)) * u);
        }
      const __amdgpu_buffer_rsrc_t rd = mkrsrc(wd + (size_t)fidx * 32 * D, 32 * D * 2);
      const int dbase = rs * (D * 2) + c8 * 16;
#pragma unroll 1
      for (int jl = w; jl < 10; jl += 4) {
        u32x4 t[4];
#pragma unroll
        for (int rg = 0; rg < 4; ++rg) t[rg] = bload<2>(rd, dbase + jl * 128, rg * 8 * (D * 2));
        float fa[32];
#pragma unroll
        for (int bb = 0; bb < 4; ++bb)
#pragma unroll
          for (int e = 0; e < 4; ++e) {
            fa[bb * 8 + 2 * e] = hr[bb][0] * bflo(t[0][e]) + hr[bb][1] * bflo(t[1][e]) + hr[bb][2] * bflo(t[2][e]) + hr[bb][3] * bflo(t[3][e]);
            fa[bb * 8 + 2 * e + 1] = hr[bb][0] * bfhi(t[0][e]) + hr[bb][1] * bfhi(t[1][e]) + hr[bb][2] * bfhi(t[2][e]) + hr[bb][3] * bfhi(t[3][e]);
          }
        slot_reduce<32>(fa, lane);
        *(GAS f32x4*)(ws + WS_F + ((rs >> 1) * 48 + fidx) * 640 + jl * 64 + c8 * 8 + (rs & 1) * 4) = f32x4{fa[0], fa[1], fa[2], fa[3]};
      }
    }
  }
  STAMP(4);
  wg_barrier();
  STAMP(5);
  {
    const f32x4 ml0 = *(LAS f32x4*)(lds + L_ML), ml1 = *(LAS f32x4*)(lds + L_ML + 4);
    const float M = fmaxf(fmaxf(ml0[0], ml0[2]), fmaxf(ml1[0], ml1[2]));
    float e[4] = {__builtin_amdgcn_exp2f(ml0[0] - M), __builtin_amdgcn_exp2f(ml0[2] - M), __builtin_amdgcn_exp2f(ml1[0] - M), __builtin_amdgcn_exp2f(ml1[2] - M)};
    const float L = e[0] * ml0[1] + e[1] * ml0[3] + e[2] * ml1[1] + e[3] * ml1[3];
    const float inv = 1.f / L;
    if (tid == 0) *(GAS f32x2*)(ws + WS_ML + slot * 2) = f32x2{M, L};
    unsigned op[3];
    const int r0 = 48 * (w >> 1) + rs;
#pragma unroll
    for (int p = 0; p < 3; ++p) {
      float o0 = 0.f, o1 = 0.f;
#pragma unroll
      for (int sw = 0; sw < 4; ++sw) {
        o0 = fmaf(e[sw], lds[L_O + sw * 192 + r0 + 16 * p], o0);
        o1 = fmaf(e[sw], lds[L_O + sw * 192 + r0 + 16 * p + 8], o1);
      }
      op[p] = pk2(o0 * inv, o1 * inv);
    }
#pragma unroll
    for (int jl = 0; jl < 5; ++jl) {
      float ya[8];
#pragma unroll
      for (int k = 0; k < 8; ++k) {
        ya[k] = dot2(wf[jl * 3][k >> 2][k & 3], op[0], 0.f);
        ya[k] = dot2(wf[jl * 3 + 1][k >> 2][k & 3], op[1], ya[k]);
        ya[k] = dot2(wf[jl * 3 + 2][k >> 2][k & 3], op[2], ya[k]);
      }
      slot_reduce<8>(ya, lane);
      lds[L_RED + (w >> 1) * 640 + (w & 1) * 320 + jl * 64 + c8 * 8 + rs] = ya[0];
    }
  }
  wg_barrier();
  if (tid < 320) {
    f32x2 t = *(LAS f32x2*)(lds + L_RED + tid * 2);
#pragma unroll
    for (int r = 1; r < 4; ++r) t += *(LAS f32x2*)(lds + L_RED + r * 640 + tid * 2);
    *(GAS f32x2*)(ws + WS_Y + slot * 640 + tid * 2) = t;
  }
}

__global__ void __launch_bounds__(64)
k2_mix(const unsigned short* __restrict__ x, const unsigned short* __restrict__ gw, const float* __restrict__ ws,
       unsigned short* __restrict__ out) {
  const int lane = threadIdx.x, b = blockIdx.x / 40, c0 = (blockIdx.x % 40) * 16;
  const int pc = lane & 3, v0 = lane >> 2;
  f32x4 y[4], f[3], ml[4];
#pragma unroll
  for (int i = 0; i < 4; ++i) {
    const int v = b * 64 + v0 + 16 * i;
    y[i] = *(const GAS f32x4*)(ws + WS_Y + v * 640 + c0 + pc * 4);
    if (i < 3) f[i] = *(const GAS f32x4*)(ws + WS_F + (b * 48 + v0 + 16 * i) * 640 + c0 + pc * 4);
    ml[i] = *(const GAS f32x4*)(ws + WS_ML + (v & ~1) * 2);
  }
  const u32x4 xa = *(const GAS u32x4*)(x + b * D + lane * 8), ga = *(const GAS u32x4*)(gw + lane * 8);
  const int l2 = 64 + (lane & 15);
  const u32x4 xb = *(const GAS u32x4*)(x + b * D + l2 * 8), gb = *(const GAS u32x4*)(gw + l2 * 8);
  SB();
  float gs = 0.f, gs2 = 0.f;
#pragma unroll
  for (int e = 0; e < 4; ++e) {
    gs += rbf(bflo(xa[e]) * bflo(ga[e])) + rbf(bfhi(xa[e]) * bfhi(ga[e]));
    gs2 += rbf(bflo(xb[e]) * bflo(gb[e])) + rbf(bfhi(xb[e]) * bfhi(gb[e]));
  }
  gs += lane < 16 ? gs2 : 0.f;
  gs += dpp<XOR1>(gs); gs += dpp<XOR2>(gs); gs += dpp<HMIR>(gs);
  gs = xsum_8_16_32(gs);
  const float g = rbf(1.f / (1.f + __expf(-rbf(gs))));
  f32x4 a = {0.f, 0.f, 0.f, 0.f}, bb = {0.f, 0.f, 0.f, 0.f};
#pragma unroll
  for (int i = 0; i < 4; ++i) {
    const float M = fmaxf(ml[i][0], ml[i][2]);
    const float w0 = __builtin_amdgcn_exp2f(ml[i][0] - M) * ml[i][1], w1 = __builtin_amdgcn_exp2f(ml[i][2] - M) * ml[i][3];
    const float wt = ((v0 & 1) ? w1 : w0) / (w0 + w1);
    a += wt * y[i];
    if (i < 3) bb += f[i];
  }
  float r[8] = {a[0], a[1], a[2], a[3], bb[0], bb[1], bb[2], bb[3]};
#pragma unroll
  for (int k = 0; k < 8; ++k) {
    float t = r[k];
    t += __shfl_xor(t, 4);
    r[k] = xsum_8_16_32(t);
  }
  if (lane < 4) {
    float o[4];
#pragma unroll
    for (int k = 0; k < 4; ++k) o[k] = rbf(g * rbf(r[k])) + rbf(rbf(1.f - g) * rbf(r[4 + k]));
    *(GAS u32x2*)(out + b * D + c0 + pc * 4) = u32x2{pk2(o[0], o[1]), pk2(o[2], o[3])};
  }
}
}

extern "C" void kernel_launch(void* const* d_in, const int* in_sizes, int n_in, void* d_out, int out_size, void* d_ws,
                              size_t ws_size, hipStream_t stream) {
  const unsigned short* x = (const unsigned short*)d_in[0];
  const unsigned short* kc = (const unsigned short*)d_in[1];
  const unsigned short* vc = (const unsigned short*)d_in[2];
  const unsigned short* wq = (const unsigned short*)d_in[3];
  const unsigned short* wo = (const unsigned short*)d_in[4];
  const unsigned short* wg = (const unsigned short*)d_in[6];
  const unsigned short* wu = (const unsigned short*)d_in[7];
  const unsigned short* wd = (const unsigned short*)d_in[8];
  const unsigned short* gw = (const unsigned short*)d_in[9];
  float* ws = (float*)d_ws;
  hipLaunchKernelGGL(k1_attn, dim3(256), dim3(NT), 0, stream, x, kc, vc, wq, wo, wg, wu, wd, ws);
  hipLaunchKernelGGL(k2_mix, dim3(160), dim3(64), 0, stream, x, gw, (const float*)ws, (unsigned short*)d_out);
}
```

```cpp
#include <hip/hip_runtime.h>
#include <hip/hip_bf16.h>
#include <stdint.h>

#define GAS __attribute__((address_space(1)))
#define LAS __attribute__((address_space(3)))
typedef unsigned u32x4 __attribute__((ext_vector_type(4)));
typedef unsigned u32x2 __attribute__((ext_vector_type(2)));
typedef float f32x4 __attribute__((ext_vector_type(4)));
typedef float f32x2 __attribute__((ext_vector_type(2)));
typedef __bf16 bf16x2 __attribute__((ext_vector_type(2)));

#define RING 8
#define AUXQ 0
#define AUXW 0
#define ROTM 37

#define WB 1
#define WB2 1
#define LATE_OWN 1
#define PF_WO 1
#define AUXP 0
#define STP 0
#define DELTA 12
#define BAR2 64
#define ABL_Q 0
#define ABL_O 0
#define ABL_F 0
namespace {
constexpr int H = 32, SK = 8192, DH = 192, D = 640, DF = 1536;
constexpr int NT = 512;
constexpr int ROWB = DH * 2;
constexpr int GRP_B = 8 * ROWB;
constexpr int NTILE = SK / 2 / 8;
constexpr int PER = NTILE / 4;
constexpr int WS_Y = 0;
constexpr int WS_F = 256 * 640;
constexpr int WS_ML = 2 * 256 * 640;

__device__ __forceinline__ float dot2(unsigned a, unsigned b, float acc) {
  return __builtin_amdgcn_fdot2_f32_bf16(__builtin_bit_cast(bf16x2, a), __builtin_bit_cast(bf16x2, b), acc, false);
}
template <int CTRL> __device__ __forceinline__ float dpp(float x) {
  return __builtin_bit_cast(float, __builtin_amdgcn_mov_dpp(__builtin_bit_cast(int, x), CTRL, 0xf, 0xf, true));
}
constexpr int XOR1 = 0xB1, XOR2 = 0x4E, HMIR = 0x141, ROR8 = 0x128;
__device__ __forceinline__ float bflo(unsigned u) { return __uint_as_float(u << 16); }
__device__ __forceinline__ float bfhi(unsigned u) { return __uint_as_float(u & 0xffff0000u); }
__device__ __forceinline__ float bf2f(unsigned short u) { return __uint_as_float((unsigned)u << 16); }
__device__ __forceinline__ unsigned short f2bf(float f) { return __builtin_bit_cast(unsigned short, (__bf16)f); }
__device__ __forceinline__ float rbf(float f) { return bf2f(f2bf(f)); }
__device__ __forceinline__ unsigned pk2(float lo, float hi) { return (unsigned)f2bf(lo) | ((unsigned)f2bf(hi) << 16); }
__device__ __forceinline__ unsigned plo(unsigned r0, unsigned r1) { return __builtin_amdgcn_perm(r1, r0, 0x05040100u); }
__device__ __forceinline__ unsigned phi(unsigned r0, unsigned r1) { return __builtin_amdgcn_perm(r1, r0, 0x07060302u); }
__device__ __forceinline__ float swap32_add(float a, float b) {
  auto r = __builtin_amdgcn_permlane32_swap(__float_as_uint(a), __float_as_uint(b), false, false);
  return __uint_as_float(r[0]) + __uint_as_float(r[1]);
}
__device__ __forceinline__ float swap16_add(float a, float b) {
  auto r = __builtin_amdgcn_permlane16_swap(__float_as_uint(a), __float_as_uint(b), false, false);
  return __uint_as_float(r[0]) + __uint_as_float(r[1]);
}
__device__ __forceinline__ float xmax_8_16_32(float x) {
  x = fmaxf(x, dpp<ROR8>(x));
  auto s = __builtin_amdgcn_permlane16_swap(__float_as_uint(x), __float_as_uint(x), false, false);
  x = fmaxf(__uint_as_float(s[0]), __uint_as_float(s[1]));
  auto t = __builtin_amdgcn_permlane32_swap(__float_as_uint(x), __float_as_uint(x), false, false);
  return fmaxf(__uint_as_float(t[0]), __uint_as_float(t[1]));
}
__device__ __forceinline__ float xsum_8_16_32(float x) {
  x = x + dpp<ROR8>(x);
  x = swap16_add(x, x);
  return swap32_add(x, x);
}
template <int N> __device__ __forceinline__ void slot_reduce(float (&v)[N], int lane) {
#pragma unroll
  for (int j = 0; j < N / 2; ++j) v[j] = swap32_add(v[j], v[j + N / 2]);
#pragma unroll
  for (int j = 0; j < N / 4; ++j) v[j] = swap16_add(v[j], v[j + N / 4]);
#pragma unroll
  for (int j = 0; j < N / 8; ++j) {
    const float a = v[j] + dpp<ROR8>(v[j]), b = v[j + N / 8] + dpp<ROR8>(v[j + N / 8]);
    v[j] = (lane & 8) ? b : a;
  }
}
template <int AUX> __device__ __forceinline__ u32x4 bload(__amdgpu_buffer_rsrc_t r, int voff, int soff = 0) {
  return __builtin_amdgcn_raw_buffer_load_b128(r, voff, soff, AUX);
}
__device__ __forceinline__ __amdgpu_buffer_rsrc_t mkrsrc(const void* p, unsigned bytes) {
  return __builtin_amdgcn_make_buffer_rsrc((void*)p, 0, (int)bytes, 0x00020000);
}
#define SB() __builtin_amdgcn_sched_barrier(0)
#define PIN8(a, o) asm volatile("" : "+v"(a[o + 0]), "+v"(a[o + 1]), "+v"(a[o + 2]), "+v"(a[o + 3]), "+v"(a[o + 4]), "+v"(a[o + 5]), "+v"(a[o + 6]), "+v"(a[o + 7]) :: "memory")
#define PIN24(a) asm volatile("" : "+v"(a[0]), "+v"(a[1]), "+v"(a[2]), "+v"(a[3]), "+v"(a[4]), "+v"(a[5]), "+v"(a[6]), "+v"(a[7]), \
  "+v"(a[8]), "+v"(a[9]), "+v"(a[10]), "+v"(a[11]), "+v"(a[12]), "+v"(a[13]), "+v"(a[14]), "+v"(a[15]), \
  "+v"(a[16]), "+v"(a[17]), "+v"(a[18]), "+v"(a[19]), "+v"(a[20]), "+v"(a[21]), "+v"(a[22]), "+v"(a[23]) :: "memory")
#define STAMP(i)
__device__ __forceinline__ void wg_barrier() {
  asm volatile("s_waitcnt lgkmcnt(0)\n\ts_barrier" ::: "memory");
}

constexpr int L_W = 0;
constexpr int L_Q = 4 * 15 * 2 * 256;
constexpr int L_O = L_Q + 4 * 192;
constexpr int L_ML = L_O + 4 * 192;
constexpr int L_RED = L_ML + 8;
constexpr int L_G = L_RED + 4 * 640;
constexpr int L_S = L_G + 4 * 256;
constexpr int L_END = L_S + 4 * 1024;

__global__ void __launch_bounds__(NT, 2)
k1_attn(const unsigned short* __restrict__ x, const unsigned short* __restrict__ kc, const unsigned short* __restrict__ vc,
        const unsigned short* __restrict__ wq, const unsigned short* __restrict__ wo, const unsigned short* __restrict__ wg,
        const unsigned short* __restrict__ wu, const unsigned short* __restrict__ wd, float* __restrict__ ws) {
  __shared__ __attribute__((aligned(16))) float lds_[L_END];
  LAS float* lds = (LAS float*)lds_;
  const int tid = threadIdx.x, lane = tid & 63, w = __builtin_amdgcn_readfirstlane(tid >> 6);
  const int rs = lane >> 3, c8 = lane & 7;
  const int bid = blockIdx.x, xg = bid & 7, j = bid >> 3;
  const int hl = j & 3, h = xg * 4 + hl, mem = j >> 2, b = mem >> 1, s = mem & 1;
  const int slot = b * 64 + h * 2 + s;
  const int fl = b * 4 + hl;
  const bool fpair = (xg & 1) == 0 && fl < 12;
  const bool ffn = fpair && s == 0;
  const int fidx = (xg >> 1) * 12 + fl;
  STAMP(0);
  u32x4 wf[15][2];

  if (w >= 4) {
    const int sw = w - 4;
    const size_t kvbase = ((size_t)(b * H + h) * SK + (size_t)s * (SK / 2)) * DH;
    const __amdgpu_buffer_rsrc_t rk = mkrsrc(kc + kvbase, NTILE * GRP_B);
    const __amdgpu_buffer_rsrc_t rv = mkrsrc(vc + kvbase, NTILE * GRP_B);
    const __amdgpu_buffer_rsrc_t rz = mkrsrc(vc + kvbase, 0);
    const int loff = rs * ROWB + c8 * 16;
    int tl = (bid * ROTM + sw) & (NTILE - 1);
    u32x4 rb[16][3];
#define ISSUE(u, rsrc) do { const int so = tl * GRP_B; tl = (tl + 4) & (NTILE - 1); \
      _Pragma("unroll") for (int i = 0; i < 3; ++i) { rb[u][i] = bload<2>(rsrc, loff + i * 128, so); SB(); } } while (0)
#pragma unroll
    for (int u = 0; u < 16; ++u) ISSUE(u, rk);
    STAMP(1);
    wg_barrier();
    u32x4 qf[3];
#pragma unroll
    for (int i = 0; i < 3; ++i) {
      f32x4 t0 = {0.f, 0.f, 0.f, 0.f}, t1 = {0.f, 0.f, 0.f, 0.f};
#pragma unroll
      for (int hw = 0; hw < 4; ++hw) {
        t0 += *(LAS f32x4*)(lds + L_Q + hw * 192 + i * 64 + c8 * 8);
        t1 += *(LAS f32x4*)(lds + L_Q + hw * 192 + i * 64 + c8 * 8 + 4);
      }
      qf[i] = u32x4{pk2(t0[0], t0[1]), pk2(t0[2], t0[3]), pk2(t1[0], t1[1]), pk2(t1[2], t1[3])};
    }
    STAMP(2);
    constexpr float C2 = 0.07216878364870322f * 1.4426950408889634f;
    LAS float* scw = lds + L_S + sw * (PER * 8);
    float m = -INFINITY;
    for (int it = 0; it < PER / 16; ++it) {
      const __amdgpu_buffer_rsrc_t rn = it < PER / 16 - 1 ? rk : rv;
#pragma unroll
      for (int g = 0; g < 2; ++g) {
        float x[8];
#pragma unroll
        for (int t = 0; t < 8; ++t) {
          const int u = g * 8 + t;
          float s0 = 0.f, s1 = 0.f;
#pragma unroll
          for (int i = 0; i < 3; ++i) {
            s0 = dot2(rb[u][i][0], qf[i][0], s0); s1 = dot2(rb[u][i][1], qf[i][1], s1);
            s0 = dot2(rb[u][i][2], qf[i][2], s0); s1 = dot2(rb[u][i][3], qf[i][3], s1);
          }
          asm volatile("" : "+v"(s0), "+v"(s1) :: "memory");
          SB();
          ISSUE(u, rn);
          x[t] = s0 + s1;
        }
#pragma unroll
        for (int t = 0; t < 4; ++t) { const float keep = (c8 & 4) ? x[t + 4] : x[t], send = (c8 & 4) ? x[t] : x[t + 4]; x[t] = keep + dpp<HMIR>(send); }
#pragma unroll
        for (int t = 0; t < 2; ++t) { const float keep = (c8 & 2) ? x[t + 2] : x[t], send = (c8 & 2) ? x[t] : x[t + 2]; x[t] = keep + dpp<XOR2>(send); }
        { const float keep = (c8 & 1) ? x[1] : x[0], send = (c8 & 1) ? x[0] : x[1]; x[0] = keep + dpp<XOR1>(send); }
        const float sc = x[0] * C2;
        m = fmaxf(m, sc);
        scw[(it * 2 + g) * 64 + lane] = sc;
      }
    }
    wg_barrier();
    m = fmaxf(m, dpp<XOR1>(m)); m = fmaxf(m, dpp<XOR2>(m)); m = fmaxf(m, dpp<HMIR>(m));
    const float M = xmax_8_16_32(m);
    float l = 0.f;
#pragma unroll
    for (int g = 0; g < PER / 8; ++g) {
      const float p = __builtin_amdgcn_exp2f(scw[g * 64 + lane] - M);
      l += p;
      scw[g * 64 + lane] = p;
    }
    float acc[24];
#pragma unroll
    for (int k = 0; k < 24; ++k) acc[k] = 0.f;
    for (int it = 0; it < PER / 16; ++it) {
      const __amdgpu_buffer_rsrc_t rn = it < PER / 16 - 1 ? rv : rz;
#pragma unroll
      for (int g = 0; g < 2; ++g) {
        const f32x4 p0 = *(LAS f32x4*)(scw + (it * 2 + g) * 64 + rs * 8), p1 = *(LAS f32x4*)(scw + (it * 2 + g) * 64 + rs * 8 + 4);
#pragma unroll
        for (int t = 0; t < 8; ++t) {
          const int u = g * 8 + t;
          const float p = t < 4 ? p0[t & 3] : p1[t & 3];
#pragma unroll
          for (int i = 0; i < 3; ++i)
#pragma unroll
            for (int e = 0; e < 4; ++e) {
              acc[i * 8 + 2 * e] = fmaf(p, bflo(rb[u][i][e]), acc[i * 8 + 2 * e]);
              acc[i * 8 + 2 * e + 1] = fmaf(p, bfhi(rb[u][i][e]), acc[i * 8 + 2 * e + 1]);
            }
          PIN24(acc);
          SB();
          ISSUE(u, rn);
        }
      }
    }
#undef ISSUE
    STAMP(3);
#pragma unroll
    for (int pj = 0; pj < 15; ++pj) {
      wf[pj][0] = *(LAS u32x4*)(lds + L_W + ((sw * 15 + pj) * 2 + 0) * 256 + lane * 4);
      wf[pj][1] = *(LAS u32x4*)(lds + L_W + ((sw * 15 + pj) * 2 + 1) * 256 + lane * 4);
    }
    l += dpp<XOR1>(l); l += dpp<XOR2>(l); l += dpp<HMIR>(l);
    const float L = xsum_8_16_32(l);
    slot_reduce<24>(acc, lane);
#pragma unroll
    for (int jj = 0; jj < 3; ++jj) { const int k = 3 * rs + jj; lds[L_O + sw * 192 + (k >> 3) * 64 + c8 * 8 + (k & 7)] = acc[jj]; }
    if (lane == 0) *(LAS f32x2*)(lds + L_ML + 2 * sw) = f32x2{M, L};
  } else {
    {
      const __amdgpu_buffer_rsrc_t rq = mkrsrc(wq, ABL_Q ? 0 : D * H * DH * 2);
      const __amdgpu_buffer_rsrc_t rx = mkrsrc(x, 4 * D * 2);
      const int row0 = 160 * w + 20 * rs;
      const int qo0 = row0 * (H * DH * 2) + h * ROWB + c8 * 16, qo1 = qo0 + H * DH * 2;
      const int gp0 = (mem * 5) >> 2;
      unsigned xp[10];
      u32x4 wr[8][6];
#pragma unroll
      for (int g = 0; g < 10; ++g) {
        int gq = g + gp0; gq = gq >= 10 ? gq - 10 : gq;
        xp[g] = __builtin_amdgcn_raw_buffer_load_b32(rx, (b * D + row0) * 2, gq * 4, 0);
      }
      SB();
#pragma unroll
      for (int g = 0; g < 8; ++g) {
        int gq = g + gp0; gq = gq >= 10 ? gq - 10 : gq;
#pragma unroll
        for (int i = 0; i < 3; ++i) { wr[g][i] = bload<AUXQ>(rq, qo0 + i * 128, gq * (2 * H * DH * 2)); SB(); }
#pragma unroll
        for (int i = 0; i < 3; ++i) { wr[g][3 + i] = bload<AUXQ>(rq, qo1 + i * 128, gq * (2 * H * DH * 2)); SB(); }
      }
      float a[24];
#pragma unroll
      for (int k = 0; k < 24; ++k) a[k] = 0.f;
#pragma unroll
      for (int g = 0; g < 10; ++g) {
#pragma unroll
        for (int i = 0; i < 3; ++i)
#pragma unroll
          for (int e = 0; e < 4; ++e) {
            a[i * 8 + 2 * e] = dot2(plo(wr[g & 7][i][e], wr[g & 7][3 + i][e]), xp[g], a[i * 8 + 2 * e]);
            a[i * 8 + 2 * e + 1] = dot2(phi(wr[g & 7][i][e], wr[g & 7][3 + i][e]), xp[g], a[i * 8 + 2 * e + 1]);
          }
        PIN24(a);
        SB();
        if (g < 2) {
          int gq = g + 8 + gp0; gq = gq >= 10 ? gq - 10 : gq;
#pragma unroll
          for (int i = 0; i < 3; ++i) { wr[g][i] = bload<AUXQ>(rq, qo0 + i * 128, gq * (2 * H * DH * 2)); SB(); }
#pragma unroll
          for (int i = 0; i < 3; ++i) { wr[g][3 + i] = bload<AUXQ>(rq, qo1 + i * 128, gq * (2 * H * DH * 2)); SB(); }
        }
      }
      slot_reduce<24>(a, lane);
#pragma unroll
      for (int jj = 0; jj < 3; ++jj) { const int k = 3 * rs + jj; lds[L_Q + w * 192 + (k >> 3) * 64 + c8 * 8 + (k & 7)] = a[jj]; }
    }
    STAMP(1);
    wg_barrier();
    const __amdgpu_buffer_rsrc_t ro = mkrsrc(wo + (size_t)h * DH * D, ABL_O ? 0 : DH * D * 2);
    {
      u32x4 t[8];
#pragma unroll
      for (int i = 0; i < 8; ++i) t[i] = bload<AUXP>(ro, (i == 7 && lane >= 32) ? 0x40000000 : mem * 30720 + w * 7680 + i * 1024 + lane * 16);
#pragma unroll
      for (int i = 0; i < 8; ++i) asm volatile("" :: "v"(t[i]) : "memory");
      SB();
    }
    {
      const int tw = 4 + w;
      const int obase = (48 * (tw >> 1) + rs) * (D * 2) + (tw & 1) * 640 + c8 * 16;
#pragma unroll 1
      for (int jl = 0; jl < 5; ++jl) {
#pragma unroll
        for (int p0 = 0; p0 < 3; p0 += WB) {
          u32x4 t[WB][2];
#pragma unroll
          for (int p = 0; p < WB; ++p) {
            t[p][0] = bload<AUXW>(ro, obase + jl * 128, (16 * (p0 + p)) * (D * 2));
            t[p][1] = bload<AUXW>(ro, obase + jl * 128, (16 * (p0 + p) + 8) * (D * 2));
          }
#pragma unroll
          for (int p = 0; p < WB; ++p) {
            LAS u32x4* dst = (LAS u32x4*)(lds + L_W + ((w * 15 + jl * 3 + p0 + p) * 2) * 256 + lane * 4);
            dst[0] = u32x4{plo(t[p][0][0], t[p][1][0]), phi(t[p][0][0], t[p][1][0]), plo(t[p][0][1], t[p][1][1]), phi(t[p][0][1], t[p][1][1])};
            dst[64] = u32x4{plo(t[p][0][2], t[p][1][2]), phi(t[p][0][2], t[p][1][2]), plo(t[p][0][3], t[p][1][3]), phi(t[p][0][3], t[p][1][3])};
          }
          asm volatile("" ::: "memory");
          SB();
        }
      }
    }
    STAMP(2);
#define OWN_WO() do { \
      const int obase = (48 * (w >> 1) + rs) * (D * 2) + (w & 1) * 640 + c8 * 16; \
      _Pragma("unroll") for (int q0 = 0; q0 < 15; q0 += WB2) { \
        u32x4 t[WB2][2]; \
        _Pragma("unroll") for (int q = 0; q < WB2; ++q) { \
          t[q][0] = bload<AUXW>(ro, obase + ((q0 + q) / 3) * 128, (16 * ((q0 + q) % 3)) * (D * 2)); \
          t[q][1] = bload<AUXW>(ro, obase + ((q0 + q) / 3) * 128, (16 * ((q0 + q) % 3) + 8) * (D * 2)); } \
        _Pragma("unroll") for (int q = 0; q < WB2; ++q) { \
          wf[q0 + q][0] = u32x4{plo(t[q][0][0], t[q][1][0]), phi(t[q][0][0], t[q][1][0]), plo(t[q][0][1], t[q][1][1]), phi(t[q][0][1], t[q][1][1])}; \
          wf[q0 + q][1] = u32x4{plo(t[q][0][2], t[q][1][2]), phi(t[q][0][2], t[q][1][2]), plo(t[q][0][3], t[q][1][3]), phi(t[q][0][3], t[q][1][3])}; \
          asm volatile("" : "+v"(wf[q0 + q][0]), "+v"(wf[q0 + q][1]) :: "memory"); } \
        SB(); } } while (0)
    if (ffn && !ABL_F) {
      const int row0 = 160 * w + 20 * rs;
      const GAS unsigned short* gp = (const GAS unsigned short*)(c8 < 4 ? wg : wu) + (size_t)row0 * DF + fidx * 32 + (c8 & 3) * 8;
      const GAS unsigned* xq = (const GAS unsigned*)(x + row0);
      float ga[32];
#pragma unroll
      for (int k = 0; k < 32; ++k) ga[k] = 0.f;
#pragma unroll 1
      for (int g = 0; g < 5; ++g) {
        u32x4 t[4];
        u32x2 xx[4];
#pragma unroll
        for (int r = 0; r < 4; ++r) t[r] = __builtin_nontemporal_load((const GAS u32x4*)(gp + (size_t)(g * 4 + r) * DF));
#pragma unroll
        for (int bb = 0; bb < 4; ++bb) xx[bb] = *(const GAS u32x2*)(xq + bb * (D / 2) + g * 2);
#pragma unroll
        for (int r = 0; r < 2; ++r)
#pragma unroll
          for (int e = 0; e < 4; ++e) {
            const unsigned lo = plo(t[2 * r][e], t[2 * r + 1][e]), hi = phi(t[2 * r][e], t[2 * r + 1][e]);
#pragma unroll
            for (int bb = 0; bb < 4; ++bb) {
              ga[bb * 8 + 2 * e] = dot2(lo, xx[bb][r], ga[bb * 8 + 2 * e]);
              ga[bb * 8 + 2 * e + 1] = dot2(hi, xx[bb][r], ga[bb * 8 + 2 * e + 1]);
            }
          }
      }
      slot_reduce<32>(ga, lane);
      *(LAS f32x4*)(lds + L_G + w * 256 + (rs >> 1) * 64 + c8 * 8 + (rs & 1) * 4) = f32x4{ga[0], ga[1], ga[2], ga[3]};
    }
    STAMP(3);
    wg_barrier();
    if (ffn && !ABL_F) {
      float hr[4][4];
#pragma unroll
      for (int bb = 0; bb < 4; ++bb)
#pragma unroll
        for (int rg = 0; rg < 4; ++rg) {
          float g = 0.f, u = 0.f;
#pragma unroll
          for (int hw = 0; hw < 4; ++hw) { g += lds[L_G + hw * 256 + bb * 64 + 8 * rg + rs]; u += lds[L_G + hw * 256 + bb * 64 + 32 + 8 * rg + rs]; }
          g = rbf(g); u = rbf(u);
          const float inner = 0.7978845608028654f * (g + 0.044715f * g * g * g);
          const float th = 1.f - 2.f / (1.f + __expf(2.f * inner));
          hr[bb][rg] = rbf(rbf(0.5f * g * (1.f + th)) * u);
        }
      const __amdgpu_buffer_rsrc_t rd = mkrsrc(wd + (size_t)fidx * 32 * D, 32 * D * 2);
      const int dbase = rs * (D * 2) + c8 * 16;
#pragma unroll 1
      for (int jl = w; jl < 10; jl += 4) {
        u32x4 t[4];
#pragma unroll
        for (int rg = 0; rg < 4; ++rg) t[rg] = bload<2>(rd, dbase + jl * 128, rg * 8 * (D * 2));
        float fa[32];
#pragma unroll
        for (int bb = 0; bb < 4; ++bb)
#pragma unroll
          for (int e = 0; e < 4; ++e) {
            fa[bb * 8 + 2 * e] = hr[bb][0] * bflo(t[0][e]) + hr[bb][1] * bflo(t[1][e]) + hr[bb][2] * bflo(t[2][e]) + hr[bb][3] * bflo(t[3][e]);
            fa[bb * 8 + 2 * e + 1] = hr[bb][0] * bfhi(t[0][e]) + hr[bb][1] * bfhi(t[1][e]) + hr[bb][2] * bfhi(t[2][e]) + hr[bb][3] * bfhi(t[3][e]);
          }
        slot_reduce<32>(fa, lane);
        *(GAS f32x4*)(ws + WS_F + ((rs >> 1) * 48 + fidx) * 640 + jl * 64 + c8 * 8 + (rs & 1) * 4) = f32x4{fa[0], fa[1], fa[2], fa[3]};
      }
    }
    OWN_WO();
  }
  STAMP(4);
  wg_barrier();
  STAMP(5);
  {
    const f32x4 ml0 = *(LAS f32x4*)(lds + L_ML), ml1 = *(LAS f32x4*)(lds + L_ML + 4);
    const float M = fmaxf(fmaxf(ml0[0], ml0[2]), fmaxf(ml1[0], ml1[2]));
    float e[4] = {__builtin_amdgcn_exp2f(ml0[0] - M), __builtin_amdgcn_exp2f(ml0[2] - M), __builtin_amdgcn_exp2f(ml1[0] - M), __builtin_amdgcn_exp2f(ml1[2] - M)};
    const float L = e[0] * ml0[1] + e[1] * ml0[3] + e[2] * ml1[1] + e[3] * ml1[3];
    const float inv = 1.f / L;
    if (tid == 0) *(GAS f32x2*)(ws + WS_ML + slot * 2) = f32x2{M, L};
    unsigned op[3];
    const int r0 = 48 * (w >> 1) + rs;
#pragma unroll
    for (int p = 0; p < 3; ++p) {
      float o0 = 0.f, o1 = 0.f;
#pragma unroll
      for (int sw = 0; sw < 4; ++sw) {
        o0 = fmaf(e[sw], lds[L_O + sw * 192 + r0 + 16 * p], o0);
        o1 = fmaf(e[sw], lds[L_O + sw * 192 + r0 + 16 * p + 8], o1);
      }
      op[p] = pk2(o0 * inv, o1 * inv);
    }
#pragma unroll
    for (int jl = 0; jl < 5; ++jl) {
      float ya[8];
#pragma unroll
      for (int k = 0; k < 8; ++k) {
        ya[k] = dot2(wf[jl * 3][k >> 2][k & 3], op[0], 0.f);
        ya[k] = dot2(wf[jl * 3 + 1][k >> 2][k & 3], op[1], ya[k]);
        ya[k] = dot2(wf[jl * 3 + 2][k >> 2][k & 3], op[2], ya[k]);
      }
      slot_reduce<8>(ya, lane);
      lds[L_RED + (w >> 1) * 640 + (w & 1) * 320 + jl * 64 + c8 * 8 + rs] = ya[0];
    }
  }
  wg_barrier();
  if (tid < 320) {
    f32x2 t = *(LAS f32x2*)(lds + L_RED + tid * 2);
#pragma unroll
    for (int r = 1; r < 4; ++r) t += *(LAS f32x2*)(lds + L_RED + r * 640 + tid * 2);
    *(GAS f32x2*)(ws + WS_Y + slot * 640 + tid * 2) = t;
  }
}

__global__ void __launch_bounds__(64)
k2_mix(const unsigned short* __restrict__ x, const unsigned short* __restrict__ gw, const float* __restrict__ ws,
       unsigned short* __restrict__ out) {
  const int lane = threadIdx.x, b = blockIdx.x / 40, c0 = (blockIdx.x % 40) * 16;
  const int pc = lane & 3, v0 = lane >> 2;
  f32x4 y[4], f[3], ml[4];
#pragma unroll
  for (int i = 0; i < 4; ++i) {
    const int v = b * 64 + v0 + 16 * i;
    y[i] = *(const GAS f32x4*)(ws + WS_Y + v * 640 + c0 + pc * 4);
    if (i < 3) f[i] = *(const GAS f32x4*)(ws + WS_F + (b * 48 + v0 + 16 * i) * 640 + c0 + pc * 4);
    ml[i] = *(const GAS f32x4*)(ws + WS_ML + (v & ~1) * 2);
  }
  const u32x4 xa = *(const GAS u32x4*)(x + b * D + lane * 8), ga = *(const GAS u32x4*)(gw + lane * 8);
  const int l2 = 64 + (lane & 15);
  const u32x4 xb = *(const GAS u32x4*)(x + b * D + l2 * 8), gb = *(const GAS u32x4*)(gw + l2 * 8);
  SB();
  float gs = 0.f, gs2 = 0.f;
#pragma unroll
  for (int e = 0; e < 4; ++e) {
    gs += rbf(bflo(xa[e]) * bflo(ga[e])) + rbf(bfhi(xa[e]) * bfhi(ga[e]));
    gs2 += rbf(bflo(xb[e]) * bflo(gb[e])) + rbf(bfhi(xb[e]) * bfhi(gb[e]));
  }
  gs += lane < 16 ? gs2 : 0.f;
  gs += dpp<XOR1>(gs); gs += dpp<XOR2>(gs); gs += dpp<HMIR>(gs);
  gs = xsum_8_16_32(gs);
  const float g = rbf(1.f / (1.f + __expf(-rbf(gs))));
  f32x4 a = {0.f, 0.f, 0.f, 0.f}, bb = {0.f, 0.f, 0.f, 0.f};
#pragma unroll
  for (int i = 0; i < 4; ++i) {
    const float M = fmaxf(ml[i][0], ml[i][2]);
    const float w0 = __builtin_amdgcn_exp2f(ml[i][0] - M) * ml[i][1], w1 = __builtin_amdgcn_exp2f(ml[i][2] - M) * ml[i][3];
    const float wt = ((v0 & 1) ? w1 : w0) / (w0 + w1);
    a += wt * y[i];
    if (i < 3) bb += f[i];
  }
  float r[8] = {a[0], a[1], a[2], a[3], bb[0], bb[1], bb[2], bb[3]};
#pragma unroll
  for (int k = 0; k < 8; ++k) {
    float t = r[k];
    t += __shfl_xor(t, 4);
    r[k] = xsum_8_16_32(t);
  }
  if (lane < 4) {
    float o[4];
#pragma unroll
    for (int k = 0; k < 4; ++k) o[k] = rbf(g * rbf(r[k])) + rbf(rbf(1.f - g) * rbf(r[4 + k]));
    *(GAS u32x2*)(out + b * D + c0 + pc * 4) = u32x2{pk2(o[0], o[1]), pk2(o[2], o[3])};
  }
}
}

extern "C" void kernel_launch(void* const* d_in, const int* in_sizes, int n_in, void* d_out, int out_size, void* d_ws,
                              size_t ws_size, hipStream_t stream) {
  const unsigned short* x = (const unsigned short*)d_in[0];
  const unsigned short* kc = (const unsigned short*)d_in[1];
  const unsigned short* vc = (const unsigned short*)d_in[2];
  const unsigned short* wq = (const unsigned short*)d_in[3];
  const unsigned short* wo = (const unsigned short*)d_in[4];
  const unsigned short* wg = (const unsigned short*)d_in[6];
  const unsigned short* wu = (const unsigned short*)d_in[7];
  const unsigned short* wd = (const unsigned short*)d_in[8];
  const unsigned short* gw = (const unsigned short*)d_in[9];
  float* ws = (float*)d_ws;
  hipLaunchKernelGGL(k1_attn, dim3(256), dim3(NT), 0, stream, x, kc, vc, wq, wo, wg, wu, wd, ws);
  hipLaunchKernelGGL(k2_mix, dim3(160), dim3(64), 0, stream, x, gw, (const float*)ws, (unsigned short*)d_out);
}
```

```cpp
#include <hip/hip_runtime.h>
#include <hip/hip_bf16.h>
#include <stdint.h>

#define GAS __attribute__((address_space(1)))
#define LAS __attribute__((address_space(3)))
typedef unsigned u32x4 __attribute__((ext_vector_type(4)));
typedef unsigned u32x3 __attribute__((ext_vector_type(3)));
typedef unsigned u32x2 __attribute__((ext_vector_type(2)));
typedef float f32x4 __attribute__((ext_vector_type(4)));
typedef float f32x2 __attribute__((ext_vector_type(2)));
typedef __bf16 bf16x2 __attribute__((ext_vector_type(2)));
typedef __bf16 bf16x8 __attribute__((ext_vector_type(8)));

#define ROTM 37
#define ROTV 0
#define QRING 5
#define VREV 1
#define ABL_Q 0
#define ABL_O 0
#define ABL_F 0

namespace {
constexpr int H = 32, SK = 8192, DH = 192, D = 640, DF = 1536;
constexpr int NT = 512;
constexpr int ROWB = DH * 2;
constexpr int TILE_B = 8 * ROWB;
constexpr int WS_Y = 0;
constexpr int WS_F = 4 * 160 * 256 * 4;
constexpr int WS_ML = WS_F + 4 * 160 * 48 * 4;
constexpr int WS_G = WS_ML + 4 * 256 * 2;

__device__ __forceinline__ float dot2(unsigned a, unsigned b, float acc) {
  return __builtin_amdgcn_fdot2_f32_bf16(__builtin_bit_cast(bf16x2, a), __builtin_bit_cast(bf16x2, b), acc, false);
}
__device__ __forceinline__ f32x4 mfma(u32x4 a, u32x4 b, f32x4 c) {
  return __builtin_amdgcn_mfma_f32_16x16x32_bf16(__builtin_bit_cast(bf16x8, a), __builtin_bit_cast(bf16x8, b), c, 0, 0, 0);
}
template <int CTRL> __device__ __forceinline__ float dpp(float x) {
  return __builtin_bit_cast(float, __builtin_amdgcn_mov_dpp(__builtin_bit_cast(int, x), CTRL, 0xf, 0xf, true));
}
constexpr int XOR1 = 0xB1, XOR2 = 0x4E, HMIR = 0x141, ROR8 = 0x128;
__device__ __forceinline__ float bflo(unsigned u) { return __uint_as_float(u << 16); }
__device__ __forceinline__ float bfhi(unsigned u) { return __uint_as_float(u & 0xffff0000u); }
__device__ __forceinline__ float bf2f(unsigned short u) { return __uint_as_float((unsigned)u << 16); }
__device__ __forceinline__ unsigned short f2bf(float f) { return __builtin_bit_cast(unsigned short, (__bf16)f); }
__device__ __forceinline__ float rbf(float f) { return bf2f(f2bf(f)); }
__device__ __forceinline__ unsigned pk2(float lo, float hi) { return (unsigned)f2bf(lo) | ((unsigned)f2bf(hi) << 16); }
__device__ __forceinline__ unsigned plo(unsigned r0, unsigned r1) { return __builtin_amdgcn_perm(r1, r0, 0x05040100u); }
__device__ __forceinline__ unsigned phi(unsigned r0, unsigned r1) { return __builtin_amdgcn_perm(r1, r0, 0x07060302u); }
__device__ __forceinline__ float swap32_add(float a, float b) {
  auto r = __builtin_amdgcn_permlane32_swap(__float_as_uint(a), __float_as_uint(b), false, false);
  return __uint_as_float(r[0]) + __uint_as_float(r[1]);
}
__device__ __forceinline__ float swap16_add(float a, float b) {
  auto r = __builtin_amdgcn_permlane16_swap(__float_as_uint(a), __float_as_uint(b), false, false);
  return __uint_as_float(r[0]) + __uint_as_float(r[1]);
}
__device__ __forceinline__ float max8(float x) { x = fmaxf(x, dpp<XOR1>(x)); x = fmaxf(x, dpp<XOR2>(x)); return fmaxf(x, dpp<HMIR>(x)); }
__device__ __forceinline__ float sum8(float x) { x += dpp<XOR1>(x); x += dpp<XOR2>(x); return x + dpp<HMIR>(x); }
__device__ __forceinline__ float wave_max(float x) {
  x = max8(x);
  x = fmaxf(x, dpp<ROR8>(x));
  auto s = __builtin_amdgcn_permlane16_swap(__float_as_uint(x), __float_as_uint(x), false, false);
  x = fmaxf(__uint_as_float(s[0]), __uint_as_float(s[1]));
  auto t = __builtin_amdgcn_permlane32_swap(__float_as_uint(x), __float_as_uint(x), false, false);
  return fmaxf(__uint_as_float(t[0]), __uint_as_float(t[1]));
}
__device__ __forceinline__ float wave_sum(float x) {
  x = sum8(x);
  x = x + dpp<ROR8>(x);
  x = swap16_add(x, x);
  return swap32_add(x, x);
}
template <int N> __device__ __forceinline__ void slot_reduce(float (&v)[N], int lane) {
#pragma unroll
  for (int j = 0; j < N / 2; ++j) v[j] = swap32_add(v[j], v[j + N / 2]);
#pragma unroll
  for (int j = 0; j < N / 4; ++j) v[j] = swap16_add(v[j], v[j + N / 4]);
#pragma unroll
  for (int j = 0; j < N / 8; ++j) {
    const float a = v[j] + dpp<ROR8>(v[j]), b = v[j + N / 8] + dpp<ROR8>(v[j + N / 8]);
    v[j] = (lane & 8) ? b : a;
  }
}
template <int AUX> __device__ __forceinline__ u32x4 bload(__amdgpu_buffer_rsrc_t r, int voff, int soff = 0) {
  return __builtin_amdgcn_raw_buffer_load_b128(r, voff, soff, AUX);
}
__device__ __forceinline__ __amdgpu_buffer_rsrc_t mkrsrc(const void* p, unsigned bytes) {
  return __builtin_amdgcn_make_buffer_rsrc((void*)p, 0, (int)bytes, 0x00020000);
}
#define SB() __builtin_amdgcn_sched_barrier(0)
#define PIN24(a, o) asm volatile("" : "+v"(a[o + 0]), "+v"(a[o + 1]), "+v"(a[o + 2]), "+v"(a[o + 3]), "+v"(a[o + 4]), "+v"(a[o + 5]), "+v"(a[o + 6]), "+v"(a[o + 7]), \
  "+v"(a[o + 8]), "+v"(a[o + 9]), "+v"(a[o + 10]), "+v"(a[o + 11]), "+v"(a[o + 12]), "+v"(a[o + 13]), "+v"(a[o + 14]), "+v"(a[o + 15]), \
  "+v"(a[o + 16]), "+v"(a[o + 17]), "+v"(a[o + 18]), "+v"(a[o + 19]), "+v"(a[o + 20]), "+v"(a[o + 21]), "+v"(a[o + 22]), "+v"(a[o + 23]) :: "memory")
#define STAMP(i)
__device__ __forceinline__ void wg_barrier() {
  asm volatile("s_waitcnt lgkmcnt(0)\n\ts_barrier" ::: "memory");
}

struct RowPair { u32x3 a0, a1; u32x2 b0, b1; };
__device__ __forceinline__ RowPair frag_load(__amdgpu_buffer_rsrc_t r, int va, int vb, int soff) {
  RowPair p;
  p.a0 = __builtin_amdgcn_raw_buffer_load_b96(r, va, soff, 0);
  p.b0 = __builtin_amdgcn_raw_buffer_load_b64(r, vb, soff, 0);
  p.a1 = __builtin_amdgcn_raw_buffer_load_b96(r, va + D * 2, soff, 0);
  p.b1 = __builtin_amdgcn_raw_buffer_load_b64(r, vb + D * 2, soff, 0);
  return p;
}
__device__ __forceinline__ unsigned frag_dword(const RowPair& p, int e) {
  if (e < 6) return (e & 1) ? phi(p.a0[e >> 1], p.a1[e >> 1]) : plo(p.a0[e >> 1], p.a1[e >> 1]);
  return (e & 1) ? phi(p.b0[(e - 6) >> 1], p.b1[(e - 6) >> 1]) : plo(p.b0[(e - 6) >> 1], p.b1[(e - 6) >> 1]);
}

constexpr int L_W = 0;
constexpr int L_S = 4 * 30 * 256;
constexpr int L_Q = L_S + 4 * 1024;
constexpr int OSTR = 100;
constexpr int L_G = L_Q + 4 * 4 * 192;
constexpr int L_M = L_G + 4 * 256;
constexpr int L_L = L_M + 16;
constexpr int L_END = L_L + 16;

__global__ void __launch_bounds__(NT, 2)
k1_attn(const unsigned short* __restrict__ x, const unsigned short* __restrict__ kc, const unsigned short* __restrict__ vc,
        const unsigned short* __restrict__ wq, const unsigned short* __restrict__ wo, const unsigned short* __restrict__ wg,
        const unsigned short* __restrict__ wu, const unsigned short* __restrict__ wd, const unsigned short* __restrict__ gw,
        float* __restrict__ ws) {
  __shared__ __attribute__((aligned(16))) float lds_[L_END];
  LAS float* lds = (LAS float*)lds_;
  const int tid = threadIdx.x, lane = tid & 63, w = __builtin_amdgcn_readfirstlane(tid >> 6);
  const int rs = lane >> 3, c8 = lane & 7;
  const int bid = blockIdx.x, xg = bid & 7, j = bid >> 3;
  const int h = xg * 4 + (j & 3), mem = j >> 2;
  const int vec = h * 8 + mem;
  STAMP(0);
  u32x4 wf[6][5];

  if (w >= 4) {
    const int sw = w - 4;
    const __amdgpu_buffer_rsrc_t rk = mkrsrc(kc, 4u * H * SK * ROWB);
    const __amdgpu_buffer_rsrc_t rv = mkrsrc(vc, 4u * H * SK * ROWB);
    const int loff = rs * ROWB + c8 * 16;
    const int t0 = (ROTV ? vec : bid) * ROTM + sw;
    u32x4 rb[16][3];
#define VR(r) (VREV ? (r) ^ 6 : (r))
#define ISSUE(u, rsrc, r) do { const int so = ((((r) >> 1) * H + h) * 1024 + mem * 128 + ((t0 + ((r) & 1) * 64 + 4 * (u)) & 127)) * TILE_B; \
      _Pragma("unroll") for (int i = 0; i < 3; ++i) { rb[u][i] = bload<2>(rsrc, loff + i * 128, so); SB(); } } while (0)
#pragma unroll
    for (int u = 0; u < 16; ++u) ISSUE(u, rk, 0);
    STAMP(1);
    wg_barrier();
    STAMP(2);
    constexpr float C2 = 0.07216878364870322f * 1.4426950408889634f;
    LAS float* scw = lds + L_S + sw * 1024;
    u32x4 qf[3];
    float m = -INFINITY;
#pragma unroll 1
    for (int r = 0; r < 8; ++r) {
      if ((r & 1) == 0) {
#pragma unroll
        for (int i = 0; i < 3; ++i) {
          f32x4 a0 = {0.f, 0.f, 0.f, 0.f}, a1 = {0.f, 0.f, 0.f, 0.f};
#pragma unroll
          for (int hw = 0; hw < 4; ++hw) {
            a0 += *(LAS f32x4*)(lds + L_Q + (hw * 4 + (r >> 1)) * 192 + i * 64 + c8 * 8);
            a1 += *(LAS f32x4*)(lds + L_Q + (hw * 4 + (r >> 1)) * 192 + i * 64 + c8 * 8 + 4);
          }
          qf[i] = u32x4{pk2(a0[0], a0[1]), pk2(a0[2], a0[3]), pk2(a1[0], a1[1]), pk2(a1[2], a1[3])};
        }
        m = -INFINITY;
      }
      const __amdgpu_buffer_rsrc_t rn = r < 7 ? rk : rv;
      const int rr = r < 7 ? r + 1 : VR(0);
#pragma unroll
      for (int g = 0; g < 2; ++g) {
        float xs[8];
#pragma unroll
        for (int t = 0; t < 8; ++t) {
          const int u = g * 8 + t;
          float s0 = 0.f, s1 = 0.f;
#pragma unroll
          for (int i = 0; i < 3; ++i) {
            s0 = dot2(rb[u][i][0], qf[i][0], s0); s1 = dot2(rb[u][i][1], qf[i][1], s1);
            s0 = dot2(rb[u][i][2], qf[i][2], s0); s1 = dot2(rb[u][i][3], qf[i][3], s1);
          }
          asm volatile("" : "+v"(s0), "+v"(s1) :: "memory");
          SB();
          ISSUE(u, rn, rr);
          xs[t] = s0 + s1;
        }
#pragma unroll
        for (int t = 0; t < 4; ++t) { const float keep = (c8 & 4) ? xs[t + 4] : xs[t], send = (c8 & 4) ? xs[t] : xs[t + 4]; xs[t] = keep + dpp<HMIR>(send); }
#pragma unroll
        for (int t = 0; t < 2; ++t) { const float keep = (c8 & 2) ? xs[t + 2] : xs[t], send = (c8 & 2) ? xs[t] : xs[t + 2]; xs[t] = keep + dpp<XOR2>(send); }
        { const float keep = (c8 & 1) ? xs[1] : xs[0], send = (c8 & 1) ? xs[0] : xs[1]; xs[0] = keep + dpp<XOR1>(send); }
        const float sc = xs[0] * C2;
        m = fmaxf(m, sc);
        scw[(r * 2 + g) * 64 + lane] = sc;
      }
      if (r & 1) { const float mw = wave_max(m); if (lane == 0) lds[L_M + (r >> 1) * 4 + sw] = mw; }
    }
    wg_barrier();
#pragma unroll
    for (int bb = 0; bb < 4; ++bb) {
      const f32x4 mm = *(LAS f32x4*)(lds + L_M + bb * 4);
      const float M = fmaxf(fmaxf(mm[0], mm[1]), fmaxf(mm[2], mm[3]));
      float l = 0.f;
#pragma unroll
      for (int g = 0; g < 4; ++g) {
        const float p = __builtin_amdgcn_exp2f(scw[(bb * 4 + g) * 64 + lane] - M);
        l += p;
        scw[(bb * 4 + g) * 64 + lane] = p;
      }
      l = wave_sum(l);
      if (lane == 0) lds[L_L + bb * 4 + sw] = l;
    }
    float acc[24];
#pragma unroll
    for (int k = 0; k < 24; ++k) acc[k] = 0.f;
#define V_ROUND(r, MORE) do { \
      _Pragma("unroll") for (int g = 0; g < 2; ++g) { \
        const f32x4 p0 = *(LAS f32x4*)(scw + (VR(r) * 2 + g) * 64 + rs * 8), p1 = *(LAS f32x4*)(scw + (VR(r) * 2 + g) * 64 + rs * 8 + 4); \
        _Pragma("unroll") for (int t = 0; t < 8; ++t) { \
          const int u = g * 8 + t; \
          const float p = t < 4 ? p0[t & 3] : p1[t & 3]; \
          _Pragma("unroll") for (int i = 0; i < 3; ++i) \
            _Pragma("unroll") for (int e = 0; e < 4; ++e) { \
              acc[i * 8 + 2 * e] = fmaf(p, bflo(rb[u][i][e]), acc[i * 8 + 2 * e]); \
              acc[i * 8 + 2 * e + 1] = fmaf(p, bfhi(rb[u][i][e]), acc[i * 8 + 2 * e + 1]); } \
          PIN24(acc, 0); \
          SB(); \
          if (MORE) ISSUE(u, rv, VR((r) + 1)); \
          else if (u < 10) { _Pragma("unroll") for (int i = 0; i < 3; ++i) \
              wf[(u * 3 + i) / 5][(u * 3 + i) % 5] = *(LAS u32x4*)(lds + L_W + (sw * 30 + u * 3 + i) * 256 + lane * 4); SB(); } \
        } } } while (0)
#define V_FLUSH(bb) do { \
      slot_reduce<24>(acc, lane); \
      LAS unsigned short* ob = (LAS unsigned short*)(lds + L_Q + ((bb) * 4 + sw) * OSTR); \
      _Pragma("unroll") for (int jj = 0; jj < 3; ++jj) { const int k = 3 * rs + jj; ob[(k >> 3) * 64 + c8 * 8 + (k & 7)] = f2bf(acc[jj]); } } while (0)
#pragma unroll 1
    for (int r = 0; r < 7; ++r) {
      if (r == 6) wg_barrier();
      V_ROUND(r, true);
      if (r & 1) {
        V_FLUSH(VR(r) >> 1);
#pragma unroll
        for (int k = 0; k < 24; ++k) acc[k] = 0.f;
      }
    }
    V_ROUND(7, false);
    STAMP(3);
    V_FLUSH(VR(7) >> 1);
#undef V_ROUND
#undef V_FLUSH
#undef ISSUE
  } else {
    const int row0 = 160 * w + 20 * rs;
    const __amdgpu_buffer_rsrc_t rx = mkrsrc(x, 4 * D * 2);
    {
      const __amdgpu_buffer_rsrc_t rq = mkrsrc(wq, ABL_Q ? 0 : D * H * DH * 2);
      const int qo0 = row0 * (H * DH * 2) + h * ROWB + c8 * 16, qo1 = qo0 + H * DH * 2;
      const int gp0 = (mem * 5) >> 2;
      constexpr int QR = QRING;
      u32x4 wr[QR][6];
      unsigned xp[QR][4];
#define Q_ISSUE(g_) do { int gq = (g_) + gp0; gq = gq >= 10 ? gq - 10 : gq; \
        _Pragma("unroll") for (int bb = 0; bb < 4; ++bb) { xp[(g_) % QR][bb] = __builtin_amdgcn_raw_buffer_load_b32(rx, row0 * 2 + bb * (D * 2), gq * 4, 0); SB(); } \
        _Pragma("unroll") for (int i = 0; i < 3; ++i) { wr[(g_) % QR][i] = bload<0>(rq, qo0 + i * 128, gq * (2 * H * DH * 2)); SB(); \
          wr[(g_) % QR][3 + i] = bload<0>(rq, qo1 + i * 128, gq * (2 * H * DH * 2)); SB(); } } while (0)
#pragma unroll
      for (int g = 0; g < QR; ++g) Q_ISSUE(g);
      float a[96];
#pragma unroll
      for (int k = 0; k < 96; ++k) a[k] = 0.f;
#pragma unroll
      for (int g = 0; g < 10; ++g) {
#pragma unroll
        for (int i = 0; i < 3; ++i)
#pragma unroll
          for (int e = 0; e < 4; ++e) {
            const unsigned lo = plo(wr[g % QR][i][e], wr[g % QR][3 + i][e]), hi = phi(wr[g % QR][i][e], wr[g % QR][3 + i][e]);
#pragma unroll
            for (int bb = 0; bb < 4; ++bb) {
              a[bb * 24 + i * 8 + 2 * e] = dot2(lo, xp[g % QR][bb], a[bb * 24 + i * 8 + 2 * e]);
              a[bb * 24 + i * 8 + 2 * e + 1] = dot2(hi, xp[g % QR][bb], a[bb * 24 + i * 8 + 2 * e + 1]);
            }
          }
        PIN24(a, 0); PIN24(a, 24); PIN24(a, 48); PIN24(a, 72);
        SB();
        if (g + QR < 10) Q_ISSUE(g + QR);
      }
#undef Q_ISSUE
      slot_reduce<96>(a, lane);
#pragma unroll
      for (int q4 = 0; q4 < 3; ++q4) {
        const int k = 12 * (rs & 1) + 4 * q4;
        *(LAS f32x4*)(lds + L_Q + (w * 4 + (rs >> 1)) * 192 + (k >> 3) * 64 + c8 * 8 + (k & 7)) = f32x4{a[4 * q4], a[4 * q4 + 1], a[4 * q4 + 2], a[4 * q4 + 3]};
      }
    }
    STAMP(1);
    wg_barrier();
    const __amdgpu_buffer_rsrc_t ro = mkrsrc(wo + (size_t)h * DH * D, ABL_O ? 0 : DH * D * 2);
    {
      u32x4 t[8];
#pragma unroll
      for (int i = 0; i < 8; ++i) t[i] = bload<0>(ro, (i == 7 && lane >= 32) ? 0x40000000 : mem * 30720 + w * 7680 + i * 1024 + lane * 16);
#pragma unroll
      for (int i = 0; i < 8; ++i) asm volatile("" :: "v"(t[i]) : "memory");
      SB();
    }
    const bool ffn = (xg & 1) == 0 && j < 12 && !ABL_F;
    const int fidx = (xg >> 1) * 12 + j;
    if (ffn) {
      const GAS unsigned short* gp = (const GAS unsigned short*)(c8 < 4 ? wg : wu) + (size_t)row0 * DF + fidx * 32 + (c8 & 3) * 8;
      float ga[32];
#pragma unroll
      for (int k = 0; k < 32; ++k) ga[k] = 0.f;
#pragma unroll 1
      for (int g = 0; g < 5; ++g) {
        u32x4 t[4];
        u32x2 xx[4];
#pragma unroll
        for (int r = 0; r < 4; ++r) t[r] = __builtin_nontemporal_load((const GAS u32x4*)(gp + (size_t)(g * 4 + r) * DF));
#pragma unroll
        for (int bb = 0; bb < 4; ++bb) xx[bb] = __builtin_amdgcn_raw_buffer_load_b64(rx, row0 * 2 + bb * (D * 2), g * 8, 0);
#pragma unroll
        for (int r = 0; r < 2; ++r)
#pragma unroll
          for (int e = 0; e < 4; ++e) {
            const unsigned lo = plo(t[2 * r][e], t[2 * r + 1][e]), hi = phi(t[2 * r][e], t[2 * r + 1][e]);
#pragma unroll
            for (int bb = 0; bb < 4; ++bb) {
              ga[bb * 8 + 2 * e] = dot2(lo, xx[bb][r], ga[bb * 8 + 2 * e]);
              ga[bb * 8 + 2 * e + 1] = dot2(hi, xx[bb][r], ga[bb * 8 + 2 * e + 1]);
            }
          }
      }
      slot_reduce<32>(ga, lane);
      *(LAS f32x4*)(lds + L_G + w * 256 + (rs >> 1) * 64 + c8 * 8 + (rs & 1) * 4) = f32x4{ga[0], ga[1], ga[2], ga[3]};
    }
    if (bid == 255) {
      const GAS unsigned short* xb_ = (const GAS unsigned short*)x + w * D;
      const u32x4 xa = *(const GAS u32x4*)(xb_ + lane * 8), ga = *(const GAS u32x4*)((const GAS unsigned short*)gw + lane * 8);
      const int l2 = 64 + (lane & 15);
      const u32x4 xb = *(const GAS u32x4*)(xb_ + l2 * 8), gb = *(const GAS u32x4*)((const GAS unsigned short*)gw + l2 * 8);
      float gs = 0.f, gs2 = 0.f;
#pragma unroll
      for (int e = 0; e < 4; ++e) {
        gs += rbf(bflo(xa[e]) * bflo(ga[e])) + rbf(bfhi(xa[e]) * bfhi(ga[e]));
        gs2 += rbf(bflo(xb[e]) * bflo(gb[e])) + rbf(bfhi(xb[e]) * bfhi(gb[e]));
      }
      gs = wave_sum(gs + (lane < 16 ? gs2 : 0.f));
      if (lane == 0) ws[WS_G + w] = rbf(1.f / (1.f + __expf(-rbf(gs))));
    }
    const int va = (lane >> 4) * 8 * (D * 2) + 320 * w + (lane & 15) * 12, vb = (lane >> 4) * 8 * (D * 2) + 320 * w + 192 + (lane & 15) * 8;
#define WO_STEP(ks) do { u32x4 sf[5]; \
      _Pragma("unroll") for (int t = 0; t < 4; ++t) { \
        const RowPair p = frag_load(ro, va, vb, ((ks) * 32 + 2 * t) * (D * 2)); \
        _Pragma("unroll") for (int e = 0; e < 5; ++e) { wf[ks][e][t] = frag_dword(p, e); sf[e][t] = frag_dword(p, 5 + e); } \
        asm volatile("" : "+v"(wf[ks][0]), "+v"(wf[ks][1]), "+v"(wf[ks][2]), "+v"(wf[ks][3]), "+v"(wf[ks][4]), \
                          "+v"(sf[0]), "+v"(sf[1]), "+v"(sf[2]), "+v"(sf[3]), "+v"(sf[4]) :: "memory"); \
        SB(); } \
      _Pragma("unroll") for (int e = 0; e < 5; ++e) *(LAS u32x4*)(lds + L_W + (w * 30 + (ks) * 5 + e) * 256 + lane * 4) = sf[e]; } while (0)
    WO_STEP(0); WO_STEP(1); WO_STEP(2); WO_STEP(3);
    STAMP(2);
    wg_barrier();
    if (ffn) {
      const __amdgpu_buffer_rsrc_t rd = mkrsrc(wd + (size_t)fidx * 32 * D, 32 * D * 2);
      RowPair p[4];
#pragma unroll
      for (int t = 0; t < 4; ++t) p[t] = frag_load(rd, va, vb, 2 * t * (D * 2));
      float hv[8];
      const int go = L_G + (lane & 3) * 64 + (lane >> 4) * 8;
#pragma unroll
      for (int q = 0; q < 2; ++q) {
        f32x4 g4 = {0.f, 0.f, 0.f, 0.f}, u4 = {0.f, 0.f, 0.f, 0.f};
#pragma unroll
        for (int hw = 0; hw < 4; ++hw) { g4 += *(LAS f32x4*)(lds + go + hw * 256 + q * 4); u4 += *(LAS f32x4*)(lds + go + hw * 256 + 32 + q * 4); }
#pragma unroll
        for (int e = 0; e < 4; ++e) {
          const float g = rbf(g4[e]), u = rbf(u4[e]);
          const float inner = 0.7978845608028654f * (g + 0.044715f * g * g * g);
          const float th = 1.f - 2.f / (1.f + __expf(2.f * inner));
          hv[q * 4 + e] = rbf(0.5f * g * (1.f + th)) * u;
        }
      }
      const u32x4 hb = {pk2(hv[0], hv[1]), pk2(hv[2], hv[3]), pk2(hv[4], hv[5]), pk2(hv[6], hv[7])};
#pragma unroll
      for (int e = 0; e < 10; ++e) {
        const u32x4 af = {frag_dword(p[0], e), frag_dword(p[1], e), frag_dword(p[2], e), frag_dword(p[3], e)};
        const f32x4 d = mfma(af, hb, f32x4{0.f, 0.f, 0.f, 0.f});
        if ((lane & 15) < 4) *(GAS f32x4*)(ws + WS_F + (((lane & 3) * 160 + (w * 10 + e) * 4 + (lane >> 4)) * 48 + fidx) * 4) = d;
      }
    }
    WO_STEP(4); WO_STEP(5);
#undef WO_STEP
    STAMP(3);
    wg_barrier();
    if (tid < 4) {
      const f32x4 mm = *(LAS f32x4*)(lds + L_M + tid * 4), ll = *(LAS f32x4*)(lds + L_L + tid * 4);
      *(GAS f32x2*)(ws + WS_ML + (tid * 256 + vec) * 2) = f32x2{fmaxf(fmaxf(mm[0], mm[1]), fmaxf(mm[2], mm[3])), (ll[0] + ll[1]) + (ll[2] + ll[3])};
    }
  }
  STAMP(4);
  wg_barrier();
  STAMP(5);
  {
    u32x4 ob[6];
#pragma unroll
    for (int ks = 0; ks < 6; ++ks) ob[ks] = *(LAS u32x4*)(lds + L_Q + (lane & 15) * OSTR + ks * 16 + (lane >> 4) * 4);
    f32x4 y[5];
#pragma unroll
    for (int e = 0; e < 5; ++e) y[e] = f32x4{0.f, 0.f, 0.f, 0.f};
#pragma unroll
    for (int ks = 0; ks < 6; ++ks)
#pragma unroll
      for (int e = 0; e < 5; ++e) y[e] = mfma(wf[ks][e], ob[ks], y[e]);
    GAS float* yp = (GAS float*)ws + WS_Y + ((((lane >> 2) & 3) * 160 + ((w & 3) * 10 + (w >> 2) * 5) * 4 + (lane >> 4)) * 256 + vec) * 4;
#pragma unroll
    for (int e = 0; e < 5; ++e) {
#pragma unroll
      for (int k = 0; k < 4; ++k) { float t = y[e][k]; t += dpp<XOR1>(t); t += dpp<XOR2>(t); y[e][k] = t; }
      if ((lane & 3) == 0) *(GAS f32x4*)(yp + e * (4 * 256 * 4)) = y[e];
    }
  }
}

__global__ void __launch_bounds__(256)
k2_mix(const float* __restrict__ ws, unsigned short* __restrict__ out) {
  const int lane = threadIdx.x & 63, task = blockIdx.x * 4 + __builtin_amdgcn_readfirstlane(threadIdx.x >> 6);
  const int b = task / 160, g4 = task - b * 160;
  f32x4 y[4];
  f32x2 ml[4];
#pragma unroll
  for (int i = 0; i < 4; ++i) {
    y[i] = *(const GAS f32x4*)(ws + WS_Y + (task * 256 + i * 64 + lane) * 4);
    ml[i] = *(const GAS f32x2*)(ws + WS_ML + (b * 256 + i * 64 + lane) * 2);
  }
  f32x4 f = *(const GAS f32x4*)(ws + WS_F + (task * 48 + (lane < 48 ? lane : 0)) * 4);
  const float g = ws[WS_G + b];
  SB();
  if (lane >= 48) f = f32x4{0.f, 0.f, 0.f, 0.f};
  f32x4 a = {0.f, 0.f, 0.f, 0.f};
#pragma unroll
  for (int i = 0; i < 4; ++i) {
    const float e = __builtin_amdgcn_exp2f(ml[i][0] - max8(ml[i][0]));
    a += (e / sum8(e * ml[i][1])) * y[i];
  }
  float v[8] = {a[0], a[1], a[2], a[3], f[0], f[1], f[2], f[3]};
  slot_reduce<8>(v, lane);
  const float t = rbf(sum8(v[0]));
  auto sw = __builtin_amdgcn_permlane32_swap(__float_as_uint(t), __float_as_uint(t), false, false);
  const float o = rbf(g * __uint_as_float(sw[0])) + rbf(rbf(1.f - g) * __uint_as_float(sw[1]));
  const int c = g4 >> 2, i = c / 10, e = c - i * 10, m = (g4 & 3) * 4 + (lane >> 3);
  const int col = 160 * i + (e < 6 ? 6 * m + e : 90 + 4 * m + e);
  if (lane < 32 && (lane & 7) == 0) out[b * D + col] = f2bf(o);
}
}

extern "C" void kernel_launch(void* const* d_in, const int* in_sizes, int n_in, void* d_out, int out_size, void* d_ws,
                              size_t ws_size, hipStream_t stream) {
  const unsigned short* x = (const unsigned short*)d_in[0];
  const unsigned short* kc = (const unsigned short*)d_in[1];
  const unsigned short* vc = (const unsigned short*)d_in[2];
  const unsigned short* wq = (const unsigned short*)d_in[3];
  const unsigned short* wo = (const unsigned short*)d_in[4];
  const unsigned short* wg = (const unsigned short*)d_in[6];
  const unsigned short* wu = (const unsigned short*)d_in[7];
  const unsigned short* wd = (const unsigned short*)d_in[8];
  const unsigned short* gw = (const unsigned short*)d_in[9];
  float* ws = (float*)d_ws;
  hipLaunchKernelGGL(k1_attn, dim3(256), dim3(NT), 0, stream, x, kc, vc, wq, wo, wg, wu, wd, gw, ws);
  hipLaunchKernelGGL(k2_mix, dim3(160), dim3(256), 0, stream, (const float*)ws, (unsigned short*)d_out);
}
```

```cpp
#include <hip/hip_runtime.h>
#include <hip/hip_bf16.h>
#include <stdint.h>

#define GAS __attribute__((address_space(1)))
#define LAS __attribute__((address_space(3)))
typedef unsigned u32x4 __attribute__((ext_vector_type(4)));
typedef unsigned u32x3 __attribute__((ext_vector_type(3)));
typedef unsigned u32x2 __attribute__((ext_vector_type(2)));
typedef float f32x4 __attribute__((ext_vector_type(4)));
typedef float f32x2 __attribute__((ext_vector_type(2)));
typedef __bf16 bf16x2 __attribute__((ext_vector_type(2)));
typedef __bf16 bf16x8 __attribute__((ext_vector_type(8)));

namespace {
constexpr int H = 32, SK = 8192, DH = 192, D = 640, DF = 1536;
constexpr int NT = 512;
constexpr int ROWB = DH * 2;
constexpr int TILE_B = 8 * ROWB;
constexpr int ROTM = 37;
constexpr int WS_Y = 0;
constexpr int WS_F = 4 * 160 * 256 * 4;
constexpr int WS_ML = WS_F + 4 * 160 * 48 * 4;
constexpr int WS_G = WS_ML + 4 * 256 * 2;

__device__ __forceinline__ float dot2(unsigned a, unsigned b, float acc) {
  return __builtin_amdgcn_fdot2_f32_bf16(__builtin_bit_cast(bf16x2, a), __builtin_bit_cast(bf16x2, b), acc, false);
}
__device__ __forceinline__ f32x4 mfma(u32x4 a, u32x4 b, f32x4 c) {
  return __builtin_amdgcn_mfma_f32_16x16x32_bf16(__builtin_bit_cast(bf16x8, a), __builtin_bit_cast(bf16x8, b), c, 0, 0, 0);
}
template <int CTRL> __device__ __forceinline__ float dpp(float x) {
  return __builtin_bit_cast(float, __builtin_amdgcn_mov_dpp(__builtin_bit_cast(int, x), CTRL, 0xf, 0xf, true));
}
constexpr int XOR1 = 0xB1, XOR2 = 0x4E, HMIR = 0x141, ROR8 = 0x128;
__device__ __forceinline__ float bflo(unsigned u) { return __uint_as_float(u << 16); }
__device__ __forceinline__ float bfhi(unsigned u) { return __uint_as_float(u & 0xffff0000u); }
__device__ __forceinline__ float bf2f(unsigned short u) { return __uint_as_float((unsigned)u << 16); }
__device__ __forceinline__ unsigned short f2bf(float f) { return __builtin_bit_cast(unsigned short, (__bf16)f); }
__device__ __forceinline__ float rbf(float f) { return bf2f(f2bf(f)); }
__device__ __forceinline__ unsigned pk2(float lo, float hi) { return (unsigned)f2bf(lo) | ((unsigned)f2bf(hi) << 16); }
__device__ __forceinline__ unsigned plo(unsigned r0, unsigned r1) { return __builtin_amdgcn_perm(r1, r0, 0x05040100u); }
__device__ __forceinline__ unsigned phi(unsigned r0, unsigned r1) { return __builtin_amdgcn_perm(r1, r0, 0x07060302u); }
__device__ __forceinline__ float swap32_add(float a, float b) {
  auto r = __builtin_amdgcn_permlane32_swap(__float_as_uint(a), __float_as_uint(b), false, false);
  return __uint_as_float(r[0]) + __uint_as_float(r[1]);
}
__device__ __forceinline__ float swap16_add(float a, float b) {
  auto r = __builtin_amdgcn_permlane16_swap(__float_as_uint(a), __float_as_uint(b), false, false);
  return __uint_as_float(r[0]) + __uint_as_float(r[1]);
}
__device__ __forceinline__ float max8(float x) { x = fmaxf(x, dpp<XOR1>(x)); x = fmaxf(x, dpp<XOR2>(x)); return fmaxf(x, dpp<HMIR>(x)); }
__device__ __forceinline__ float sum8(float x) { x += dpp<XOR1>(x); x += dpp<XOR2>(x); return x + dpp<HMIR>(x); }
__device__ __forceinline__ float wave_max(float x) {
  x = max8(x);
  x = fmaxf(x, dpp<ROR8>(x));
  auto s = __builtin_amdgcn_permlane16_swap(__float_as_uint(x), __float_as_uint(x), false, false);
  x = fmaxf(__uint_as_float(s[0]), __uint_as_float(s[1]));
  auto t = __builtin_amdgcn_permlane32_swap(__float_as_uint(x), __float_as_uint(x), false, false);
  return fmaxf(__uint_as_float(t[0]), __uint_as_float(t[1]));
}
__device__ __forceinline__ float wave_sum(float x) {
  x = sum8(x);
  x = x + dpp<ROR8>(x);
  x = swap16_add(x, x);
  return swap32_add(x, x);
}
template <int N> __device__ __forceinline__ void slot_reduce(float (&v)[N], int lane) {
#pragma unroll
  for (int j = 0; j < N / 2; ++j) v[j] = swap32_add(v[j], v[j + N / 2]);
#pragma unroll
  for (int j = 0; j < N / 4; ++j) v[j] = swap16_add(v[j], v[j + N / 4]);
#pragma unroll
  for (int j = 0; j < N / 8; ++j) {
    const float a = v[j] + dpp<ROR8>(v[j]), b = v[j + N / 8] + dpp<ROR8>(v[j + N / 8]);
    v[j] = (lane & 8) ? b : a;
  }
}
template <int AUX> __device__ __forceinline__ u32x4 bload(__amdgpu_buffer_rsrc_t r, int voff, int soff = 0) {
  return __builtin_amdgcn_raw_buffer_load_b128(r, voff, soff, AUX);
}
__device__ __forceinline__ __amdgpu_buffer_rsrc_t mkrsrc(const void* p, unsigned bytes) {
  return __builtin_amdgcn_make_buffer_rsrc((void*)p, 0, (int)bytes, 0x00020000);
}
#define SB() __builtin_amdgcn_sched_barrier(0)
#define PIN24(a, o) asm volatile("" : "+v"(a[o + 0]), "+v"(a[o + 1]), "+v"(a[o + 2]), "+v"(a[o + 3]), "+v"(a[o + 4]), "+v"(a[o + 5]), "+v"(a[o + 6]), "+v"(a[o + 7]), \
  "+v"(a[o + 8]), "+v"(a[o + 9]), "+v"(a[o + 10]), "+v"(a[o + 11]), "+v"(a[o + 12]), "+v"(a[o + 13]), "+v"(a[o + 14]), "+v"(a[o + 15]), \
  "+v"(a[o + 16]), "+v"(a[o + 17]), "+v"(a[o + 18]), "+v"(a[o + 19]), "+v"(a[o + 20]), "+v"(a[o + 21]), "+v"(a[o + 22]), "+v"(a[o + 23]) :: "memory")
__device__ __forceinline__ void wg_barrier() {
  asm volatile("s_waitcnt lgkmcnt(0)\n\ts_barrier" ::: "memory");
}

struct RowPair { u32x3 a0, a1; u32x2 b0, b1; };
__device__ __forceinline__ RowPair frag_load(__amdgpu_buffer_rsrc_t r, int va, int vb, int soff) {
  RowPair p;
  p.a0 = __builtin_amdgcn_raw_buffer_load_b96(r, va, soff, 0);
  p.b0 = __builtin_amdgcn_raw_buffer_load_b64(r, vb, soff, 0);
  p.a1 = __builtin_amdgcn_raw_buffer_load_b96(r, va + D * 2, soff, 0);
  p.b1 = __builtin_amdgcn_raw_buffer_load_b64(r, vb + D * 2, soff, 0);
  return p;
}
__device__ __forceinline__ unsigned frag_dword(const RowPair& p, int e) {
  if (e < 6) return (e & 1) ? phi(p.a0[e >> 1], p.a1[e >> 1]) : plo(p.a0[e >> 1], p.a1[e >> 1]);
  return (e & 1) ? phi(p.b0[(e - 6) >> 1], p.b1[(e - 6) >> 1]) : plo(p.b0[(e - 6) >> 1], p.b1[(e - 6) >> 1]);
}

constexpr int L_W = 0;
constexpr int L_S = 4 * 30 * 256;
constexpr int L_Q = L_S + 4 * 1024;
constexpr int OSTR = 100;
constexpr int L_G = L_Q + 4 * 4 * 192;
constexpr int L_M = L_G + 4 * 256;
constexpr int L_L = L_M + 16;
constexpr int L_X = L_L + 16;
constexpr int L_END = L_X + 4 * 320;

__global__ void __launch_bounds__(NT, 2)
k1_attn(const unsigned short* __restrict__ x, const unsigned short* __restrict__ kc, const unsigned short* __restrict__ vc,
        const unsigned short* __restrict__ wq, const unsigned short* __restrict__ wo, const unsigned short* __restrict__ wg,
        const unsigned short* __restrict__ wu, const unsigned short* __restrict__ wd, const unsigned short* __restrict__ gw,
        float* __restrict__ ws) {
  __shared__ __attribute__((aligned(16))) float lds_[L_END];
  LAS float* lds = (LAS float*)lds_;
  const int tid = threadIdx.x, lane = tid & 63, w = __builtin_amdgcn_readfirstlane(tid >> 6);
  const int rs = lane >> 3, c8 = lane & 7;
  const int bid = blockIdx.x, xg = bid & 7, j = bid >> 3;
  const int h = xg * 4 + (j & 3), mem = j >> 2;
  const int vec = h * 8 + mem;
  u32x4 wf[6][5];

  if (w >= 4) {
    const int sw = w - 4;
    const __amdgpu_buffer_rsrc_t rk = mkrsrc(kc, 4u * H * SK * ROWB);
    const __amdgpu_buffer_rsrc_t rv = mkrsrc(vc, 4u * H * SK * ROWB);
    const int loff = rs * ROWB + c8 * 16;
    const int t0 = bid * ROTM + sw;
    u32x4 rb[16][3];
#define VR(r) ((r) ^ 6)
#define ISSUE(u, rsrc, r) do { const int so = ((((r) >> 1) * H + h) * 1024 + mem * 128 + ((t0 + ((r) & 1) * 64 + 4 * (u)) & 127)) * TILE_B; \
      _Pragma("unroll") for (int i = 0; i < 3; ++i) { rb[u][i] = bload<2>(rsrc, loff + i * 128, so); SB(); } } while (0)
#pragma unroll
    for (int u = 0; u < 16; ++u) ISSUE(u, rk, 0);
    wg_barrier();
    constexpr float C2 = 0.07216878364870322f * 1.4426950408889634f;
    LAS float* scw = lds + L_S + sw * 1024;
    u32x4 qf[3];
    float m = -INFINITY;
#pragma unroll 1
    for (int r = 0; r < 8; ++r) {
      if ((r & 1) == 0) {
#pragma unroll
        for (int i = 0; i < 3; ++i) {
          f32x4 a0 = {0.f, 0.f, 0.f, 0.f}, a1 = {0.f, 0.f, 0.f, 0.f};
#pragma unroll
          for (int hw = 0; hw < 4; ++hw) {
            a0 += *(LAS f32x4*)(lds + L_Q + (hw * 4 + (r >> 1)) * 192 + i * 64 + c8 * 8);
            a1 += *(LAS f32x4*)(lds + L_Q + (hw * 4 + (r >> 1)) * 192 + i * 64 + c8 * 8 + 4);
          }
          qf[i] = u32x4{pk2(a0[0], a0[1]), pk2(a0[2], a0[3]), pk2(a1[0], a1[1]), pk2(a1[2], a1[3])};
        }
        m = -INFINITY;
      }
      const __amdgpu_buffer_rsrc_t rn = r < 7 ? rk : rv;
      const int rr = r < 7 ? r + 1 : VR(0);
#pragma unroll
      for (int g = 0; g < 2; ++g) {
        float xs[8];
#pragma unroll
        for (int t = 0; t < 8; ++t) {
          const int u = g * 8 + t;
          float s0 = 0.f, s1 = 0.f;
#pragma unroll
          for (int i = 0; i < 3; ++i) {
            s0 = dot2(rb[u][i][0], qf[i][0], s0); s1 = dot2(rb[u][i][1], qf[i][1], s1);
            s0 = dot2(rb[u][i][2], qf[i][2], s0); s1 = dot2(rb[u][i][3], qf[i][3], s1);
          }
          asm volatile("" : "+v"(s0), "+v"(s1) :: "memory");
          SB();
          ISSUE(u, rn, rr);
          xs[t] = s0 + s1;
        }
#pragma unroll
        for (int t = 0; t < 4; ++t) { const float keep = (c8 & 4) ? xs[t + 4] : xs[t], send = (c8 & 4) ? xs[t] : xs[t + 4]; xs[t] = keep + dpp<HMIR>(send); }
#pragma unroll
        for (int t = 0; t < 2; ++t) { const float keep = (c8 & 2) ? xs[t + 2] : xs[t], send = (c8 & 2) ? xs[t] : xs[t + 2]; xs[t] = keep + dpp<XOR2>(send); }
        { const float keep = (c8 & 1) ? xs[1] : xs[0], send = (c8 & 1) ? xs[0] : xs[1]; xs[0] = keep + dpp<XOR1>(send); }
        const float sc = xs[0] * C2;
        m = fmaxf(m, sc);
        scw[(r * 2 + g) * 64 + lane] = sc;
      }
      if (r & 1) { const float mw = wave_max(m); if (lane == 0) lds[L_M + (r >> 1) * 4 + sw] = mw; }
    }
    wg_barrier();
#pragma unroll
    for (int bb = 0; bb < 4; ++bb) {
      const f32x4 mm = *(LAS f32x4*)(lds + L_M + bb * 4);
      const float M = fmaxf(fmaxf(mm[0], mm[1]), fmaxf(mm[2], mm[3]));
      float l = 0.f;
#pragma unroll
      for (int g = 0; g < 4; ++g) {
        const float p = __builtin_amdgcn_exp2f(scw[(bb * 4 + g) * 64 + lane] - M);
        l += p;
        scw[(bb * 4 + g) * 64 + lane] = p;
      }
      l = wave_sum(l);
      if (lane == 0) lds[L_L + bb * 4 + sw] = l;
    }
    float acc[24];
#pragma unroll
    for (int k = 0; k < 24; ++k) acc[k] = 0.f;
#define V_ROUND(r, MORE) do { \
      _Pragma("unroll") for (int g = 0; g < 2; ++g) { \
        const f32x4 p0 = *(LAS f32x4*)(scw + (VR(r) * 2 + g) * 64 + rs * 8), p1 = *(LAS f32x4*)(scw + (VR(r) * 2 + g) * 64 + rs * 8 + 4); \
        _Pragma("unroll") for (int t = 0; t < 8; ++t) { \
          const int u = g * 8 + t; \
          const float p = t < 4 ? p0[t & 3] : p1[t & 3]; \
          _Pragma("unroll") for (int i = 0; i < 3; ++i) \
            _Pragma("unroll") for (int e = 0; e < 4; ++e) { \
              acc[i * 8 + 2 * e] = fmaf(p, bflo(rb[u][i][e]), acc[i * 8 + 2 * e]); \
              acc[i * 8 + 2 * e + 1] = fmaf(p, bfhi(rb[u][i][e]), acc[i * 8 + 2 * e + 1]); } \
          PIN24(acc, 0); \
          SB(); \
          if (MORE) ISSUE(u, rv, VR((r) + 1)); \
          else if (u < 10) { _Pragma("unroll") for (int i = 0; i < 3; ++i) \
              wf[(u * 3 + i) / 5][(u * 3 + i) % 5] = *(LAS u32x4*)(lds + L_W + (sw * 30 + u * 3 + i) * 256 + lane * 4); SB(); } \
        } } } while (0)
#define V_FLUSH(bb) do { \
      slot_reduce<24>(acc, lane); \
      LAS unsigned short* ob = (LAS unsigned short*)(lds + L_Q + ((bb) * 4 + sw) * OSTR); \
      _Pragma("unroll") for (int jj = 0; jj < 3; ++jj) { const int k = 3 * rs + jj; ob[(k >> 3) * 64 + c8 * 8 + (k & 7)] = f2bf(acc[jj]); } } while (0)
#pragma unroll 1
    for (int r = 0; r < 7; ++r) {
      if (r == 6) wg_barrier();
      V_ROUND(r, true);
      if (r & 1) {
        V_FLUSH(VR(r) >> 1);
#pragma unroll
        for (int k = 0; k < 24; ++k) acc[k] = 0.f;
      }
    }
    V_ROUND(7, false);
    V_FLUSH(VR(7) >> 1);
#undef V_ROUND
#undef V_FLUSH
#undef ISSUE
  } else {
    __builtin_amdgcn_s_setprio(1);
    const int row0 = 160 * w + 20 * rs;
    LAS unsigned* xl = (LAS unsigned*)lds + L_X + w * 320;
    const u32x4 xs0 = *(const GAS u32x4*)((const GAS unsigned short*)x + (lane / 20) * D + 160 * w + (lane % 20) * 8);
    const u32x4 xs1 = *(const GAS u32x4*)((const GAS unsigned short*)x + 3 * D + 160 * w + (4 + (lane & 15)) * 8);
    SB();
    {
      const __amdgpu_buffer_rsrc_t rq = mkrsrc(wq, D * H * DH * 2);
      const int qo0 = row0 * (H * DH * 2) + h * ROWB + c8 * 16, qo1 = qo0 + H * DH * 2;
      const int gp0 = (mem * 5) >> 2;
      constexpr int QR = 5;
      u32x4 wr[QR][6];
#define Q_ISSUE(g_) do { int gq = (g_) + gp0; gq = gq >= 10 ? gq - 10 : gq; \
        _Pragma("unroll") for (int i = 0; i < 3; ++i) { wr[(g_) % QR][i] = bload<0>(rq, qo0 + i * 128, gq * (2 * H * DH * 2)); SB(); \
          wr[(g_) % QR][3 + i] = bload<0>(rq, qo1 + i * 128, gq * (2 * H * DH * 2)); SB(); } } while (0)
#pragma unroll
      for (int g = 0; g < QR; ++g) Q_ISSUE(g);
      *(LAS u32x4*)(xl + lane * 4) = xs0;
      if (lane < 16) *(LAS u32x4*)(xl + 256 + lane * 4) = xs1;
      float a[96];
#pragma unroll
      for (int k = 0; k < 96; ++k) a[k] = 0.f;
#pragma unroll
      for (int g = 0; g < 10; ++g) {
        int gx = g + gp0; gx = gx >= 10 ? gx - 10 : gx;
        unsigned xp[4];
#pragma unroll
        for (int bb = 0; bb < 4; ++bb) xp[bb] = xl[bb * 80 + rs * 10 + gx];
#pragma unroll
        for (int i = 0; i < 3; ++i)
#pragma unroll
          for (int e = 0; e < 4; ++e) {
            const unsigned lo = plo(wr[g % QR][i][e], wr[g % QR][3 + i][e]), hi = phi(wr[g % QR][i][e], wr[g % QR][3 + i][e]);
#pragma unroll
            for (int bb = 0; bb < 4; ++bb) {
              a[bb * 24 + i * 8 + 2 * e] = dot2(lo, xp[bb], a[bb * 24 + i * 8 + 2 * e]);
              a[bb * 24 + i * 8 + 2 * e + 1] = dot2(hi, xp[bb], a[bb * 24 + i * 8 + 2 * e + 1]);
            }
          }
        PIN24(a, 0); PIN24(a, 24); PIN24(a, 48); PIN24(a, 72);
        SB();
        if (g + QR < 10) Q_ISSUE(g + QR);
      }
#undef Q_ISSUE
      slot_reduce<96>(a, lane);
#pragma unroll
      for (int q4 = 0; q4 < 3; ++q4) {
        const int k = 12 * (rs & 1) + 4 * q4;
        *(LAS f32x4*)(lds + L_Q + (w * 4 + (rs >> 1)) * 192 + (k >> 3) * 64 + c8 * 8 + (k & 7)) = f32x4{a[4 * q4], a[4 * q4 + 1], a[4 * q4 + 2], a[4 * q4 + 3]};
      }
    }
    wg_barrier();
    const __amdgpu_buffer_rsrc_t ro = mkrsrc(wo + (size_t)h * DH * D, DH * D * 2);
    const bool ffn = (xg & 1) == 0 && j < 12;
    const int fidx = (xg >> 1) * 12 + j;
    if ((xg & 1) == 0 && j >= 12) {
#pragma unroll 1
      for (int i0 = 0; i0 < 6; i0 += 2) {
        u32x4 t[4];
#pragma unroll
        for (int i = 0; i < 2; ++i) {
          const int o = ((j - 12) * 32 + w * 8 + rs) * (DF * 2) + (xg >> 1) * 768 + (i0 + i) * 128 + c8 * 16;
          t[i] = bload<0>(mkrsrc(wg, D * DF * 2), o);
          t[2 + i] = bload<0>(mkrsrc(wu, D * DF * 2), o);
        }
#pragma unroll
        for (int i = 0; i < 4; ++i) asm volatile("" :: "v"(t[i]) : "memory");
      }
    }
    if (bid == 254) {
      const GAS unsigned short* xb_ = (const GAS unsigned short*)x + w * D;
      const u32x4 xa = *(const GAS u32x4*)(xb_ + lane * 8), ga = *(const GAS u32x4*)((const GAS unsigned short*)gw + lane * 8);
      const int l2 = 64 + (lane & 15);
      const u32x4 xb = *(const GAS u32x4*)(xb_ + l2 * 8), gb = *(const GAS u32x4*)((const GAS unsigned short*)gw + l2 * 8);
      float gs = 0.f, gs2 = 0.f;
#pragma unroll
      for (int e = 0; e < 4; ++e) {
        gs += rbf(bflo(xa[e]) * bflo(ga[e])) + rbf(bfhi(xa[e]) * bfhi(ga[e]));
        gs2 += rbf(bflo(xb[e]) * bflo(gb[e])) + rbf(bfhi(xb[e]) * bfhi(gb[e]));
      }
      gs = wave_sum(gs + (lane < 16 ? gs2 : 0.f));
      if (lane == 0) ws[WS_G + w] = rbf(1.f / (1.f + __expf(-rbf(gs))));
    }
    const int va = (lane >> 4) * 8 * (D * 2) + 320 * w + (lane & 15) * 12, vb = (lane >> 4) * 8 * (D * 2) + 320 * w + 192 + (lane & 15) * 8;
#define WO_STEP(ks) do { u32x4 sf[5]; \
      _Pragma("unroll") for (int t = 0; t < 4; ++t) { \
        const RowPair p = frag_load(ro, va, vb, ((ks) * 32 + 2 * t) * (D * 2)); \
        _Pragma("unroll") for (int e = 0; e < 5; ++e) { wf[ks][e][t] = frag_dword(p, e); sf[e][t] = frag_dword(p, 5 + e); } \
        asm volatile("" : "+v"(wf[ks][0]), "+v"(wf[ks][1]), "+v"(wf[ks][2]), "+v"(wf[ks][3]), "+v"(wf[ks][4]), \
                          "+v"(sf[0]), "+v"(sf[1]), "+v"(sf[2]), "+v"(sf[3]), "+v"(sf[4]) :: "memory"); \
        SB(); } \
      _Pragma("unroll") for (int e = 0; e < 5; ++e) *(LAS u32x4*)(lds + L_W + (w * 30 + (ks) * 5 + e) * 256 + lane * 4) = sf[e]; } while (0)
    WO_STEP(0); WO_STEP(1);
    if (ffn) {
      const GAS unsigned short* gp = (const GAS unsigned short*)(c8 < 4 ? wg : wu) + (size_t)(160 * w + 2 * rs) * DF + fidx * 32 + (c8 & 3) * 8;
      float ga[32];
#pragma unroll
      for (int k = 0; k < 32; ++k) ga[k] = 0.f;
#pragma unroll 1
      for (int g = 0; g < 5; ++g) {
        u32x4 t[4];
#pragma unroll
        for (int r = 0; r < 4; ++r) t[r] = *(const GAS u32x4*)(gp + (size_t)(g * 32 + (r >> 1) * 16 + (r & 1)) * DF);
#pragma unroll
        for (int r = 0; r < 2; ++r) {
          unsigned xx[4];
#pragma unroll
          for (int bb = 0; bb < 4; ++bb) xx[bb] = xl[bb * 80 + g * 16 + r * 8 + rs];
#pragma unroll
          for (int e = 0; e < 4; ++e) {
            const unsigned lo = plo(t[2 * r][e], t[2 * r + 1][e]), hi = phi(t[2 * r][e], t[2 * r + 1][e]);
#pragma unroll
            for (int bb = 0; bb < 4; ++bb) {
              ga[bb * 8 + 2 * e] = dot2(lo, xx[bb], ga[bb * 8 + 2 * e]);
              ga[bb * 8 + 2 * e + 1] = dot2(hi, xx[bb], ga[bb * 8 + 2 * e + 1]);
            }
          }
        }
      }
      slot_reduce<32>(ga, lane);
      *(LAS f32x4*)(lds + L_G + w * 256 + (rs >> 1) * 64 + c8 * 8 + (rs & 1) * 4) = f32x4{ga[0], ga[1], ga[2], ga[3]};
    }
    WO_STEP(2); WO_STEP(3);
    wg_barrier();
    if (ffn) {
      const __amdgpu_buffer_rsrc_t rd = mkrsrc(wd + (size_t)fidx * 32 * D, 32 * D * 2);
      RowPair p[4];
#pragma unroll
      for (int t = 0; t < 4; ++t) p[t] = frag_load(rd, va, vb, 2 * t * (D * 2));
      float hv[8];
      const int go = L_G + (lane & 3) * 64 + (lane >> 4) * 8;
#pragma unroll
      for (int q = 0; q < 2; ++q) {
        f32x4 g4 = {0.f, 0.f, 0.f, 0.f}, u4 = {0.f, 0.f, 0.f, 0.f};
#pragma unroll
        for (int hw = 0; hw < 4; ++hw) { g4 += *(LAS f32x4*)(lds + go + hw * 256 + q * 4); u4 += *(LAS f32x4*)(lds + go + hw * 256 + 32 + q * 4); }
#pragma unroll
        for (int e = 0; e < 4; ++e) {
          const float g = rbf(g4[e]), u = rbf(u4[e]);
          const float inner = 0.7978845608028654f * (g + 0.044715f * g * g * g);
          hv[q * 4 + e] = rbf(g / (1.f + __expf(-2.f * inner))) * u;
        }
      }
      const u32x4 hb = {pk2(hv[0], hv[1]), pk2(hv[2], hv[3]), pk2(hv[4], hv[5]), pk2(hv[6], hv[7])};
#pragma unroll
      for (int e = 0; e < 10; ++e) {
        const u32x4 af = {frag_dword(p[0], e), frag_dword(p[1], e), frag_dword(p[2], e), frag_dword(p[3], e)};
        const f32x4 d = mfma(af, hb, f32x4{0.f, 0.f, 0.f, 0.f});
        if ((lane & 15) < 4) *(GAS f32x4*)(ws + WS_F + (fidx * 640 + (lane & 3) * 160 + (w * 10 + e) * 4 + (lane >> 4)) * 4) = d;
      }
    }
    WO_STEP(4); WO_STEP(5);
#undef WO_STEP
    wg_barrier();
    if (tid < 4) {
      const f32x4 mm = *(LAS f32x4*)(lds + L_M + tid * 4), ll = *(LAS f32x4*)(lds + L_L + tid * 4);
      *(GAS f32x2*)(ws + WS_ML + (tid * 256 + vec) * 2) = f32x2{fmaxf(fmaxf(mm[0], mm[1]), fmaxf(mm[2], mm[3])), (ll[0] + ll[1]) + (ll[2] + ll[3])};
    }
  }
  wg_barrier();
  {
    u32x4 ob[6];
#pragma unroll
    for (int ks = 0; ks < 6; ++ks) ob[ks] = *(LAS u32x4*)(lds + L_Q + (lane & 15) * OSTR + ks * 16 + (lane >> 4) * 4);
    f32x4 y[5];
#pragma unroll
    for (int e = 0; e < 5; ++e) y[e] = f32x4{0.f, 0.f, 0.f, 0.f};
#pragma unroll
    for (int ks = 0; ks < 6; ++ks)
#pragma unroll
      for (int e = 0; e < 5; ++e) y[e] = mfma(wf[ks][e], ob[ks], y[e]);
    GAS float* yp = (GAS float*)ws + WS_Y + ((((lane >> 2) & 3) * 160 + ((w & 3) * 10 + (w >> 2) * 5) * 4 + (lane >> 4)) * 256 + vec) * 4;
#pragma unroll
    for (int e = 0; e < 5; ++e) {
#pragma unroll
      for (int k = 0; k < 4; ++k) { float t = y[e][k]; t += dpp<XOR1>(t); t += dpp<XOR2>(t); y[e][k] = t; }
      if ((lane & 3) == 0) *(GAS f32x4*)(yp + e * (4 * 256 * 4)) = y[e];
    }
  }
}

__global__ void __launch_bounds__(256)
k2_mix(const float* __restrict__ ws, unsigned short* __restrict__ out) {
  const int lane = threadIdx.x & 63, task = blockIdx.x * 4 + __builtin_amdgcn_readfirstlane(threadIdx.x >> 6);
  const int b = task / 160, g4 = task - b * 160;
  f32x4 y[4];
  f32x2 ml[4];
#pragma unroll
  for (int i = 0; i < 4; ++i) {
    y[i] = *(const GAS f32x4*)(ws + WS_Y + (task * 256 + i * 64 + lane) * 4);
    ml[i] = *(const GAS f32x2*)(ws + WS_ML + (b * 256 + i * 64 + lane) * 2);
  }
  f32x4 f = *(const GAS f32x4*)(ws + WS_F + ((lane < 48 ? lane : 0) * 640 + task) * 4);
  const float g = ws[WS_G + b];
  SB();
  if (lane >= 48) f = f32x4{0.f, 0.f, 0.f, 0.f};
  f32x4 a = {0.f, 0.f, 0.f, 0.f};
#pragma unroll
  for (int i = 0; i < 4; ++i) {
    const float e = __builtin_amdgcn_exp2f(ml[i][0] - max8(ml[i][0]));
    a += (e * __builtin_amdgcn_rcpf(sum8(e * ml[i][1]))) * y[i];
  }
  float v[8] = {a[0], a[1], a[2], a[3], f[0], f[1], f[2], f[3]};
  slot_reduce<8>(v, lane);
  const float t = rbf(sum8(v[0]));
  auto sw = __builtin_amdgcn_permlane32_swap(__float_as_uint(t), __float_as_uint(t), false, false);
  const float o = rbf(g * __uint_as_float(sw[0])) + rbf(rbf(1.f - g) * __uint_as_float(sw[1]));
  const int c = g4 >> 2, i = c / 10, e = c - i * 10, m = (g4 & 3) * 4 + (lane >> 3);
  const int col = 160 * i + (e < 6 ? 6 * m + e : 90 + 4 * m + e);
  if (lane < 32 && (lane & 7) == 0) out[b * D + col] = f2bf(o);
}
}

extern "C" void kernel_launch(void* const* d_in, const int* in_sizes, int n_in, void* d_out, int out_size, void* d_ws,
                              size_t ws_size, hipStream_t stream) {
  const unsigned short* x = (const unsigned short*)d_in[0];
  const unsigned short* kc = (const unsigned short*)d_in[1];
  const unsigned short* vc = (const unsigned short*)d_in[2];
  const unsigned short* wq = (const unsigned short*)d_in[3];
  const unsigned short* wo = (const unsigned short*)d_in[4];
  const unsigned short* wg = (const unsigned short*)d_in[6];
  const unsigned short* wu = (const unsigned short*)d_in[7];
  const unsigned short* wd = (const unsigned short*)d_in[8];
  const unsigned short* gw = (const unsigned short*)d_in[9];
  float* ws = (float*)d_ws;
  hipLaunchKernelGGL(k1_attn, dim3(256), dim3(NT), 0, stream, x, kc, vc, wq, wo, wg, wu, wd, gw, ws);
  hipLaunchKernelGGL(k2_mix, dim3(160), dim3(256), 0, stream, (const float*)ws, (unsigned short*)d_out);
}
```

```cpp
#include <hip/hip_runtime.h>
#include <hip/hip_bf16.h>
#include <stdint.h>

#define GAS __attribute__((address_space(1)))
#define LAS __attribute__((address_space(3)))
typedef unsigned u32x4 __attribute__((ext_vector_type(4)));
typedef unsigned u32x3 __attribute__((ext_vector_type(3)));
typedef unsigned u32x2 __attribute__((ext_vector_type(2)));
typedef float f32x4 __attribute__((ext_vector_type(4)));
typedef float f32x2 __attribute__((ext_vector_type(2)));
typedef __bf16 bf16x2 __attribute__((ext_vector_type(2)));
typedef __bf16 bf16x8 __attribute__((ext_vector_type(8)));
typedef short s16x4 __attribute__((ext_vector_type(4)));

namespace {
constexpr int H = 32, SK = 8192, DH = 192, D = 640, DF = 1536;
constexpr int NT = 512;
constexpr int ROWB = DH * 2;
constexpr int TILE_B = 8 * ROWB;
constexpr int ROTM = 37;
constexpr int WS_Y = 0;
constexpr int WS_F = 4 * 160 * 256 * 4;
constexpr int WS_ML = WS_F + 4 * 160 * 48 * 4;
constexpr int WS_G = WS_ML + 4 * 256 * 2;

__device__ __forceinline__ float dot2(unsigned a, unsigned b, float acc) {
  return __builtin_amdgcn_fdot2_f32_bf16(__builtin_bit_cast(bf16x2, a), __builtin_bit_cast(bf16x2, b), acc, false);
}
__device__ __forceinline__ f32x4 mfma(u32x4 a, u32x4 b, f32x4 c) {
  return __builtin_amdgcn_mfma_f32_16x16x32_bf16(__builtin_bit_cast(bf16x8, a), __builtin_bit_cast(bf16x8, b), c, 0, 0, 0);
}
template <int CTRL> __device__ __forceinline__ float dpp(float x) {
  return __builtin_bit_cast(float, __builtin_amdgcn_mov_dpp(__builtin_bit_cast(int, x), CTRL, 0xf, 0xf, true));
}
constexpr int XOR1 = 0xB1, XOR2 = 0x4E, HMIR = 0x141, ROR8 = 0x128;
__device__ __forceinline__ float bflo(unsigned u) { return __uint_as_float(u << 16); }
__device__ __forceinline__ float bfhi(unsigned u) { return __uint_as_float(u & 0xffff0000u); }
__device__ __forceinline__ float bf2f(unsigned short u) { return __uint_as_float((unsigned)u << 16); }
__device__ __forceinline__ unsigned short f2bf(float f) { return __builtin_bit_cast(unsigned short, (__bf16)f); }
__device__ __forceinline__ float rbf(float f) { return bf2f(f2bf(f)); }
__device__ __forceinline__ unsigned pk2(float lo, float hi) { return (unsigned)f2bf(lo) | ((unsigned)f2bf(hi) << 16); }
__device__ __forceinline__ unsigned plo(unsigned r0, unsigned r1) { return __builtin_amdgcn_perm(r1, r0, 0x05040100u); }
__device__ __forceinline__ unsigned phi(unsigned r0, unsigned r1) { return __builtin_amdgcn_perm(r1, r0, 0x07060302u); }
__device__ __forceinline__ float swap32_add(float a, float b) {
  auto r = __builtin_amdgcn_permlane32_swap(__float_as_uint(a), __float_as_uint(b), false, false);
  return __uint_as_float(r[0]) + __uint_as_float(r[1]);
}
__device__ __forceinline__ float swap16_add(float a, float b) {
  auto r = __builtin_amdgcn_permlane16_swap(__float_as_uint(a), __float_as_uint(b), false, false);
  return __uint_as_float(r[0]) + __uint_as_float(r[1]);
}
__device__ __forceinline__ float max8(float x) { x = fmaxf(x, dpp<XOR1>(x)); x = fmaxf(x, dpp<XOR2>(x)); return fmaxf(x, dpp<HMIR>(x)); }
__device__ __forceinline__ float sum8(float x) { x += dpp<XOR1>(x); x += dpp<XOR2>(x); return x + dpp<HMIR>(x); }
__device__ __forceinline__ float wave_max(float x) {
  x = max8(x);
  x = fmaxf(x, dpp<ROR8>(x));
  auto s = __builtin_amdgcn_permlane16_swap(__float_as_uint(x), __float_as_uint(x), false, false);
  x = fmaxf(__uint_as_float(s[0]), __uint_as_float(s[1]));
  auto t = __builtin_amdgcn_permlane32_swap(__float_as_uint(x), __float_as_uint(x), false, false);
  return fmaxf(__uint_as_float(t[0]), __uint_as_float(t[1]));
}
__device__ __forceinline__ float wave_sum(float x) {
  x = sum8(x);
  x = x + dpp<ROR8>(x);
  x = swap16_add(x, x);
  return swap32_add(x, x);
}
template <int N> __device__ __forceinline__ void slot_reduce(float (&v)[N], int lane) {
#pragma unroll
  for (int j = 0; j < N / 2; ++j) v[j] = swap32_add(v[j], v[j + N / 2]);
#pragma unroll
  for (int j = 0; j < N / 4; ++j) v[j] = swap16_add(v[j], v[j + N / 4]);
#pragma unroll
  for (int j = 0; j < N / 8; ++j) {
    const float a = v[j] + dpp<ROR8>(v[j]), b = v[j + N / 8] + dpp<ROR8>(v[j + N / 8]);
    v[j] = (lane & 8) ? b : a;
  }
}
template <int AUX> __device__ __forceinline__ u32x4 bload(__amdgpu_buffer_rsrc_t r, int voff, int soff = 0) {
  return __builtin_amdgcn_raw_buffer_load_b128(r, voff, soff, AUX);
}
__device__ __forceinline__ __amdgpu_buffer_rsrc_t mkrsrc(const void* p, unsigned bytes) {
  return __builtin_amdgcn_make_buffer_rsrc((void*)p, 0, (int)bytes, 0x00020000);
}
#define SB() __builtin_amdgcn_sched_barrier(0)
#define PIN24(a, o) asm volatile("" : "+v"(a[o + 0]), "+v"(a[o + 1]), "+v"(a[o + 2]), "+v"(a[o + 3]), "+v"(a[o + 4]), "+v"(a[o + 5]), "+v"(a[o + 6]), "+v"(a[o + 7]), \
  "+v"(a[o + 8]), "+v"(a[o + 9]), "+v"(a[o + 10]), "+v"(a[o + 11]), "+v"(a[o + 12]), "+v"(a[o + 13]), "+v"(a[o + 14]), "+v"(a[o + 15]), \
  "+v"(a[o + 16]), "+v"(a[o + 17]), "+v"(a[o + 18]), "+v"(a[o + 19]), "+v"(a[o + 20]), "+v"(a[o + 21]), "+v"(a[o + 22]), "+v"(a[o + 23]) :: "memory")
__device__ __forceinline__ void wg_barrier() {
  asm volatile("s_waitcnt lgkmcnt(0)\n\ts_barrier" ::: "memory");
}

struct RowPair { u32x3 a0, a1; u32x2 b0, b1; };
__device__ __forceinline__ RowPair frag_load(__amdgpu_buffer_rsrc_t r, int va, int vb, int soff) {
  RowPair p;
  p.a0 = __builtin_amdgcn_raw_buffer_load_b96(r, va, soff, 0);
  p.b0 = __builtin_amdgcn_raw_buffer_load_b64(r, vb, soff, 0);
  p.a1 = __builtin_amdgcn_raw_buffer_load_b96(r, va + D * 2, soff, 0);
  p.b1 = __builtin_amdgcn_raw_buffer_load_b64(r, vb + D * 2, soff, 0);
  return p;
}
__device__ __forceinline__ unsigned frag_dword(const RowPair& p, int e) {
  if (e < 6) return (e & 1) ? phi(p.a0[e >> 1], p.a1[e >> 1]) : plo(p.a0[e >> 1], p.a1[e >> 1]);
  return (e & 1) ? phi(p.b0[(e - 6) >> 1], p.b1[(e - 6) >> 1]) : plo(p.b0[(e - 6) >> 1], p.b1[(e - 6) >> 1]);
}

constexpr int L_W = 0;
constexpr int L_S = 4 * 30 * 256;
constexpr int L_Q = L_S + 4 * 1024;
constexpr int OSTR = 100;
constexpr int L_G = L_Q + 4 * 4 * 256;
constexpr int L_M = L_G + 4 * 256;
constexpr int L_L = L_M + 32;
constexpr int L_X = L_S - 4 * 320;
constexpr int L_END = L_L + 16;

#define LOAD_Q(qf, bb) do { _Pragma("unroll") for (int i = 0; i < 3; ++i) { \
          f32x4 a0 = {0.f, 0.f, 0.f, 0.f}, a1 = {0.f, 0.f, 0.f, 0.f}; \
          _Pragma("unroll") for (int hw = 0; hw < (i < 2 ? 4 : 8); ++hw) { \
            a0 += *(LAS f32x4*)(lds + L_Q + ((hw & 3) * 4 + (bb)) * 256 + (i + (hw >> 2)) * 64 + c8 * 8); \
            a1 += *(LAS f32x4*)(lds + L_Q + ((hw & 3) * 4 + (bb)) * 256 + (i + (hw >> 2)) * 64 + c8 * 8 + 4); } \
          qf[i] = u32x4{pk2(a0[0], a0[1]), pk2(a0[2], a0[3]), pk2(a1[0], a1[1]), pk2(a1[2], a1[3])}; } } while (0)
#define TREDUCE(xs) do { \
        _Pragma("unroll") for (int t = 0; t < 4; ++t) { const float keep = (c8 & 4) ? xs[t + 4] : xs[t], send = (c8 & 4) ? xs[t] : xs[t + 4]; xs[t] = keep + dpp<HMIR>(send); } \
        _Pragma("unroll") for (int t = 0; t < 2; ++t) { const float keep = (c8 & 2) ? xs[t + 2] : xs[t], send = (c8 & 2) ? xs[t] : xs[t + 2]; xs[t] = keep + dpp<XOR2>(send); } \
        { const float keep = (c8 & 1) ? xs[1] : xs[0], send = (c8 & 1) ? xs[0] : xs[1]; xs[0] = keep + dpp<XOR1>(send); } } while (0)
constexpr float C2 = 0.07216878364870322f * 1.4426950408889634f;

__global__ void __launch_bounds__(NT, 2)
k1_attn(const unsigned short* __restrict__ x, const unsigned short* __restrict__ kc, const unsigned short* __restrict__ vc,
        const unsigned short* __restrict__ wq, const unsigned short* __restrict__ wo, const unsigned short* __restrict__ wg,
        const unsigned short* __restrict__ wu, const unsigned short* __restrict__ wd, const unsigned short* __restrict__ gw,
        float* __restrict__ ws) {
  __shared__ __attribute__((aligned(16))) float lds_[L_END];
  LAS float* lds = (LAS float*)lds_;
  const int tid = threadIdx.x, lane = tid & 63, w = __builtin_amdgcn_readfirstlane(tid >> 6);
  const int rs = lane >> 3, c8 = lane & 7;
  const int bid = blockIdx.x, xg = bid & 7, j = bid >> 3;
  const int h = xg * 4 + (j & 3), mem = j >> 2;
  const int vec = h * 8 + mem;
  const int rel = 6 * (j & 3);
  u32x4 wf[6][5];

  if (w >= 4) {
    const int sw = w - 4;
    const __amdgpu_buffer_rsrc_t rk = mkrsrc(kc, 4u * H * SK * ROWB);
    const __amdgpu_buffer_rsrc_t rv = mkrsrc(vc, 4u * H * SK * ROWB);
    const int loff = rs * ROWB + c8 * 16;
    const int t0 = bid * ROTM + sw;
    u32x4 rb[16][3];
#define VR(r) ((r) ^ 6)
#define ISSUE_G(u, rsrc, gi) do { const int so = ((((gi) >> 2) * H + h) * 1024 + mem * 128 + ((t0 + ((gi) & 3) * 32 + 4 * ((u) & 7)) & 127)) * TILE_B; \
      _Pragma("unroll") for (int i = 0; i < 3; ++i) { rb[u][i] = bload<2>(rsrc, loff + i * 128, so); SB(); } } while (0)
#define ISSUE(u, rsrc, r) ISSUE_G(u, rsrc, (r) * 2 + ((u) >> 3))
    const __amdgpu_buffer_rsrc_t rz = mkrsrc(vc, 0);
#pragma unroll
    for (int u = 8; u < 16; ++u) { ISSUE_G(u, rk, 0); if (rel == u - 7) wg_barrier(); }
#pragma unroll
    for (int u = 0; u < 8; ++u) { ISSUE_G(u, rk, 1); if (rel == u + 9) wg_barrier(); }
#pragma unroll
    for (int u = 8; u < 16; ++u) {
#pragma unroll
      for (int i = 0; i < 3; ++i) *(LAS u32x4*)(lds + L_W + (sw * 30 + (u - 8) * 3 + i) * 256 + lane * 4) = rb[u][i];
      asm volatile("" ::: "memory");
      SB();
      ISSUE_G(u, rk, 2);
      if (rel == u + 9) wg_barrier();
    }
    wg_barrier();
    LAS float* scw = lds + L_S + sw * 1024;
    u32x4 qf[3];
    LOAD_Q(qf, 0);
    float m = -INFINITY;
#pragma unroll 1
    for (int r = 0; r < 8; ++r) {
#pragma unroll
      for (int g = 0; g < 2; ++g) {
        const int gi = 2 * r + g + 1;
        if (g == 1 && (r & 1)) {
          const float mw = wave_max(m);
          if (lane == 0) lds[L_M + (r >> 1) * 8 + sw] = mw;
          LOAD_Q(qf, (gi >> 2) & 3);
          m = -INFINITY;
        }
        const __amdgpu_buffer_rsrc_t rn = r == 7 ? rv : (gi == 14 ? rz : rk);
        const int gn = r == 7 ? VR(0) * 2 + g : (gi + 2) & 15;
        float xs[8];
#pragma unroll
        for (int t = 0; t < 8; ++t) {
          const int u = g * 8 + t;
          float s0 = 0.f, s1 = 0.f;
#pragma unroll
          for (int i = 0; i < 3; ++i) {
            s0 = dot2(rb[u][i][0], qf[i][0], s0); s1 = dot2(rb[u][i][1], qf[i][1], s1);
            s0 = dot2(rb[u][i][2], qf[i][2], s0); s1 = dot2(rb[u][i][3], qf[i][3], s1);
          }
          asm volatile("" : "+v"(s0), "+v"(s1) :: "memory");
          SB();
          ISSUE_G(u, rn, gn);
          xs[t] = s0 + s1;
        }
        TREDUCE(xs);
        const float sc = xs[0] * C2;
        m = fmaxf(m, sc);
        if (gi < 16) scw[gi * 64 + lane] = sc;
      }
    }
    wg_barrier();
#pragma unroll
    for (int bb = 0; bb < 4; ++bb) {
      const f32x4 mm = *(LAS f32x4*)(lds + L_M + bb * 8), mh = *(LAS f32x4*)(lds + L_M + bb * 8 + 4);
      const float M = fmaxf(fmaxf(fmaxf(mm[0], mm[1]), fmaxf(mm[2], mm[3])), fmaxf(fmaxf(mh[0], mh[1]), fmaxf(mh[2], mh[3])));
      float l = 0.f;
#pragma unroll
      for (int g = 0; g < 4; ++g) {
        const float p = __builtin_amdgcn_exp2f(scw[(bb * 4 + g) * 64 + lane] - M);
        l += p;
        scw[(bb * 4 + g) * 64 + lane] = p;
      }
      l = wave_sum(l);
      if (lane == 0) lds[L_L + bb * 4 + sw] = l;
    }
    float acc[24];
#pragma unroll
    for (int k = 0; k < 24; ++k) acc[k] = 0.f;
#define V_ROUND(r, MORE) do { \
      _Pragma("unroll") for (int g = 0; g < 2; ++g) { \
        const f32x4 p0 = *(LAS f32x4*)(scw + (VR(r) * 2 + g) * 64 + rs * 8), p1 = *(LAS f32x4*)(scw + (VR(r) * 2 + g) * 64 + rs * 8 + 4); \
        _Pragma("unroll") for (int t = 0; t < 8; ++t) { \
          const int u = g * 8 + t; \
          const float p = t < 4 ? p0[t & 3] : p1[t & 3]; \
          _Pragma("unroll") for (int i = 0; i < 3; ++i) \
            _Pragma("unroll") for (int e = 0; e < 4; ++e) { \
              acc[i * 8 + 2 * e] = fmaf(p, bflo(rb[u][i][e]), acc[i * 8 + 2 * e]); \
              acc[i * 8 + 2 * e + 1] = fmaf(p, bfhi(rb[u][i][e]), acc[i * 8 + 2 * e + 1]); } \
          PIN24(acc, 0); \
          SB(); \
          if (MORE) ISSUE(u, rv, VR((r) + 1)); \
          else if (u < 10) { _Pragma("unroll") for (int i = 0; i < 3; ++i) \
              wf[(u * 3 + i) / 5][(u * 3 + i) % 5] = *(LAS u32x4*)(lds + L_W + (sw * 30 + u * 3 + i) * 256 + lane * 4); SB(); } \
        } } } while (0)
#define V_FLUSH(bb) do { \
      slot_reduce<24>(acc, lane); \
      LAS unsigned short* ob = (LAS unsigned short*)(lds + L_Q + ((bb) * 4 + sw) * OSTR); \
      _Pragma("unroll") for (int jj = 0; jj < 3; ++jj) { const int k = 3 * rs + jj; ob[(k >> 3) * 64 + c8 * 8 + (k & 7)] = f2bf(acc[jj]); } } while (0)
#pragma unroll 1
    for (int r = 0; r < 7; ++r) {
      if (r == 6) wg_barrier();
      V_ROUND(r, true);
      if (r & 1) {
        V_FLUSH(VR(r) >> 1);
#pragma unroll
        for (int k = 0; k < 24; ++k) acc[k] = 0.f;
      }
    }
    V_ROUND(7, false);
    V_FLUSH(VR(7) >> 1);
#undef V_ROUND
#undef V_FLUSH
#undef ISSUE
#undef ISSUE_G
  } else {
    __builtin_amdgcn_s_setprio(1);
    {
      const __amdgpu_buffer_rsrc_t rq = mkrsrc(wq, D * H * DH * 2);
      const __amdgpu_buffer_rsrc_t rx = mkrsrc(x, 4 * D * 2);
      const int mm = lane & 15, gg = lane >> 4;
      const int qa = (160 * w + gg * 8) * (H * DH * 2) + h * ROWB + mm * 16;
      const int qb = (160 * w + (mm >> 3) * 16 + gg * 4) * (H * DH * 2) + h * ROWB + 256 + (mm & 7) * 16;
      const int xa = ((lane & 3) * D + 160 * w + gg * 8) * 2, xc = ((lane & 3) * D + 160 * w + ((mm >> 2) & 1) * 16 + gg * 4) * 2;
      const int k0 = mem >= 5 ? mem - 5 : mem;
      constexpr int QR = 4;
      u32x4 wr[QR][12], x32[QR];
      u32x2 x16[QR];
#define Q_ISSUE(s_) do { int ks = (s_) + k0; ks = ks >= 5 ? ks - 5 : ks; \
        x32[(s_) % QR] = bload<0>(rx, xa, ks * 64); SB(); \
        x16[(s_) % QR] = __builtin_amdgcn_raw_buffer_load_b64(rx, xc, ks * 64, 0); SB(); \
        _Pragma("unroll") for (int r = 0; r < 8; ++r) { wr[(s_) % QR][r] = bload<0>(rq, qa, (ks * 32 + r) * (H * DH * 2)); SB(); } \
        _Pragma("unroll") for (int r = 0; r < 4; ++r) { wr[(s_) % QR][8 + r] = bload<0>(rq, qb, (ks * 32 + r) * (H * DH * 2)); SB(); } } while (0)
      if (rel) wg_barrier();
#pragma unroll
      for (int s = 0; s < QR; ++s) Q_ISSUE(s);
      f32x4 a[6];
#pragma unroll
      for (int e = 0; e < 6; ++e) a[e] = f32x4{0.f, 0.f, 0.f, 0.f};
#pragma unroll
      for (int s = 0; s < 5; ++s) {
        const u32x4* wp = wr[s % QR];
#pragma unroll
        for (int c = 0; c < 4; ++c) {
          const u32x4 bm = (mm >> 2) == c ? x32[s % QR] : u32x4{0u, 0u, 0u, 0u};
#pragma unroll
          for (int ai = 0; ai < 2; ++ai) {
            const int e = ai * 4 + c;
            u32x4 af;
#pragma unroll
            for (int t = 0; t < 4; ++t) af[t] = (e & 1) ? phi(wp[2 * t][e >> 1], wp[2 * t + 1][e >> 1]) : plo(wp[2 * t][e >> 1], wp[2 * t + 1][e >> 1]);
            a[ai] = mfma(af, bm, a[ai]);
          }
        }
#pragma unroll
        for (int c = 0; c < 2; ++c) {
          const u32x2 bm = (mm >> 3) == c ? x16[s % QR] : u32x2{0u, 0u};
#pragma unroll
          for (int ai = 0; ai < 4; ++ai) {
            const u32x2 af = c ? u32x2{phi(wp[8][ai], wp[9][ai]), phi(wp[10][ai], wp[11][ai])} : u32x2{plo(wp[8][ai], wp[9][ai]), plo(wp[10][ai], wp[11][ai])};
            a[2 + ai] = __builtin_amdgcn_mfma_f32_16x16x16bf16_1k(__builtin_bit_cast(s16x4, af), __builtin_bit_cast(s16x4, bm), a[2 + ai], 0, 0, 0);
          }
        }
        asm volatile("" : "+v"(a[0]), "+v"(a[1]), "+v"(a[2]), "+v"(a[3]), "+v"(a[4]), "+v"(a[5]) :: "memory");
        SB();
        if (s + QR < 5) Q_ISSUE(s + QR);
      }
#undef Q_ISSUE
      LAS float* qp = lds + L_Q + (w * 4 + (lane & 3)) * 256;
#pragma unroll
      for (int ai = 0; ai < 2; ++ai)
#pragma unroll
        for (int r = 0; r < 4; ++r) qp[8 * (4 * gg + r) + 4 * ai + (mm >> 2)] = a[ai][r];
      if ((gg >> 1) == ((mm >> 2) & 1)) {
#pragma unroll
        for (int ai = 0; ai < 4; ++ai)
#pragma unroll
          for (int r = 0; r < 4; ++r) qp[128 + 8 * (4 * gg + r) + 2 * ai + (mm >> 3)] = a[2 + ai][r];
      }
    }
    wg_barrier();
    {
      u32x4 qf[3];
      LOAD_Q(qf, 0);
      float xs[8];
#pragma unroll
      for (int t = 0; t < 8; ++t) {
        float s0 = 0.f, s1 = 0.f;
#pragma unroll
        for (int i = 0; i < 3; ++i) {
          const u32x4 kk = *(LAS u32x4*)(lds + L_W + (w * 30 + t * 3 + i) * 256 + lane * 4);
          s0 = dot2(kk[0], qf[i][0], s0); s1 = dot2(kk[1], qf[i][1], s1);
          s0 = dot2(kk[2], qf[i][2], s0); s1 = dot2(kk[3], qf[i][3], s1);
        }
        xs[t] = s0 + s1;
      }
      TREDUCE(xs);
      const float sc = xs[0] * C2;
      lds[L_S + w * 1024 + lane] = sc;
      const float mw = wave_max(sc);
      if (lane < 4) lds[L_M + lane * 8 + 4 + w] = lane == 0 ? mw : -INFINITY;
    }
    const __amdgpu_buffer_rsrc_t ro = mkrsrc(wo + (size_t)h * DH * D, DH * D * 2);
    {
      u32x4 t[8];
#pragma unroll
      for (int i = 0; i < 8; ++i) t[i] = bload<0>(ro, (i == 7 && lane >= 32) ? 0x40000000 : mem * 30720 + w * 7680 + i * 1024 + lane * 16);
#pragma unroll
      for (int i = 0; i < 8; ++i) asm volatile("" :: "v"(t[i]) : "memory");
      SB();
    }
    const bool ffn = (xg & 1) == 0 && j < 12;
    const int fidx = (xg >> 1) * 12 + j;
    if ((xg & 1) == 0 && j >= 12) {
#pragma unroll 1
      for (int i0 = 0; i0 < 6; i0 += 2) {
        u32x4 t[4];
#pragma unroll
        for (int i = 0; i < 2; ++i) {
          const int o = ((j - 12) * 32 + w * 8 + rs) * (DF * 2) + (xg >> 1) * 768 + (i0 + i) * 128 + c8 * 16;
          t[i] = bload<0>(mkrsrc(wg, D * DF * 2), o);
          t[2 + i] = bload<0>(mkrsrc(wu, D * DF * 2), o);
        }
#pragma unroll
        for (int i = 0; i < 4; ++i) asm volatile("" :: "v"(t[i]) : "memory");
      }
      {
        u32x4 t[6];
#pragma unroll
        for (int i = 0; i < 6; ++i) t[i] = bload<0>(mkrsrc(wd, D * DF * 2), (xg >> 1) * 491520 + (j - 12) * 24576 + w * 6144 + i * 1024 + lane * 16);
#pragma unroll
        for (int i = 0; i < 6; ++i) asm volatile("" :: "v"(t[i]) : "memory");
      }
    }
    if (bid == 254) {
      const GAS unsigned short* xb_ = (const GAS unsigned short*)x + w * D;
      const u32x4 xa = *(const GAS u32x4*)(xb_ + lane * 8), ga = *(const GAS u32x4*)((const GAS unsigned short*)gw + lane * 8);
      const int l2 = 64 + (lane & 15);
      const u32x4 xb = *(const GAS u32x4*)(xb_ + l2 * 8), gb = *(const GAS u32x4*)((const GAS unsigned short*)gw + l2 * 8);
      float gs = 0.f, gs2 = 0.f;
#pragma unroll
      for (int e = 0; e < 4; ++e) {
        gs += rbf(bflo(xa[e]) * bflo(ga[e])) + rbf(bfhi(xa[e]) * bfhi(ga[e]));
        gs2 += rbf(bflo(xb[e]) * bflo(gb[e])) + rbf(bfhi(xb[e]) * bfhi(gb[e]));
      }
      gs = wave_sum(gs + (lane < 16 ? gs2 : 0.f));
      if (lane == 0) ws[WS_G + w] = rbf(1.f / (1.f + __expf(-rbf(gs))));
    }
    const int va = (lane >> 4) * 8 * (D * 2) + 320 * w + (lane & 15) * 12, vb = (lane >> 4) * 8 * (D * 2) + 320 * w + 192 + (lane & 15) * 8;
#define WO_STEP(ks) do { u32x4 sf[5]; \
      _Pragma("unroll") for (int t = 0; t < 4; ++t) { \
        const RowPair p = frag_load(ro, va, vb, ((ks) * 32 + 2 * t) * (D * 2)); \
        _Pragma("unroll") for (int e = 0; e < 5; ++e) { wf[ks][e][t] = frag_dword(p, e); sf[e][t] = frag_dword(p, 5 + e); } \
        asm volatile("" : "+v"(wf[ks][0]), "+v"(wf[ks][1]), "+v"(wf[ks][2]), "+v"(wf[ks][3]), "+v"(wf[ks][4]), \
                          "+v"(sf[0]), "+v"(sf[1]), "+v"(sf[2]), "+v"(sf[3]), "+v"(sf[4]) :: "memory"); \
        SB(); } \
      _Pragma("unroll") for (int e = 0; e < 5; ++e) *(LAS u32x4*)(lds + L_W + (w * 30 + (ks) * 5 + e) * 256 + lane * 4) = sf[e]; } while (0)
    WO_STEP(0); WO_STEP(1);
    if (ffn) {
      LAS unsigned* xl = (LAS unsigned*)lds + L_X + w * 320;
      {
        const u32x4 xs0 = *(const GAS u32x4*)((const GAS unsigned short*)x + (lane / 20) * D + 160 * w + (lane % 20) * 8);
        const u32x4 xs1 = *(const GAS u32x4*)((const GAS unsigned short*)x + 3 * D + 160 * w + (4 + (lane & 15)) * 8);
        *(LAS u32x4*)(xl + lane * 4) = xs0;
        if (lane < 16) *(LAS u32x4*)(xl + 256 + lane * 4) = xs1;
      }
      const GAS unsigned short* gp = (const GAS unsigned short*)(c8 < 4 ? wg : wu) + (size_t)(160 * w + 2 * rs) * DF + fidx * 32 + (c8 & 3) * 8;
      float ga[32];
#pragma unroll
      for (int k = 0; k < 32; ++k) ga[k] = 0.f;
#pragma unroll 1
      for (int g = 0; g < 5; ++g) {
        u32x4 t[4];
#pragma unroll
        for (int r = 0; r < 4; ++r) t[r] = *(const GAS u32x4*)(gp + (size_t)(g * 32 + (r >> 1) * 16 + (r & 1)) * DF);
#pragma unroll
        for (int r = 0; r < 2; ++r) {
          unsigned xx[4];
#pragma unroll
          for (int bb = 0; bb < 4; ++bb) xx[bb] = xl[bb * 80 + g * 16 + r * 8 + rs];
#pragma unroll
          for (int e = 0; e < 4; ++e) {
            const unsigned lo = plo(t[2 * r][e], t[2 * r + 1][e]), hi = phi(t[2 * r][e], t[2 * r + 1][e]);
#pragma unroll
            for (int bb = 0; bb < 4; ++bb) {
              ga[bb * 8 + 2 * e] = dot2(lo, xx[bb], ga[bb * 8 + 2 * e]);
              ga[bb * 8 + 2 * e + 1] = dot2(hi, xx[bb], ga[bb * 8 + 2 * e + 1]);
            }
          }
        }
      }
      slot_reduce<32>(ga, lane);
      *(LAS f32x4*)(lds + L_G + w * 256 + (rs >> 1) * 64 + c8 * 8 + (rs & 1) * 4) = f32x4{ga[0], ga[1], ga[2], ga[3]};
    }
    WO_STEP(2);
    WO_STEP(3);
    wg_barrier();
    if (ffn) {
      const __amdgpu_buffer_rsrc_t rd = mkrsrc(wd + (size_t)fidx * 32 * D, 32 * D * 2);
      RowPair p[4];
#pragma unroll
      for (int t = 0; t < 4; ++t) p[t] = frag_load(rd, va, vb, 2 * t * (D * 2));
      float hv[8];
      const int go = L_G + (lane & 3) * 64 + (lane >> 4) * 8;
#pragma unroll
      for (int q = 0; q < 2; ++q) {
        f32x4 g4 = {0.f, 0.f, 0.f, 0.f}, u4 = {0.f, 0.f, 0.f, 0.f};
#pragma unroll
        for (int hw = 0; hw < 4; ++hw) { g4 += *(LAS f32x4*)(lds + go + hw * 256 + q * 4); u4 += *(LAS f32x4*)(lds + go + hw * 256 + 32 + q * 4); }
#pragma unroll
        for (int e = 0; e < 4; ++e) {
          const float g = rbf(g4[e]), u = rbf(u4[e]);
          const float inner = 0.7978845608028654f * (g + 0.044715f * g * g * g);
          hv[q * 4 + e] = rbf(g / (1.f + __expf(-2.f * inner))) * u;
        }
      }
      const u32x4 hb = {pk2(hv[0], hv[1]), pk2(hv[2], hv[3]), pk2(hv[4], hv[5]), pk2(hv[6], hv[7])};
#pragma unroll
      for (int e = 0; e < 10; ++e) {
        const u32x4 af = {frag_dword(p[0], e), frag_dword(p[1], e), frag_dword(p[2], e), frag_dword(p[3], e)};
        const f32x4 d = mfma(af, hb, f32x4{0.f, 0.f, 0.f, 0.f});
        if ((lane & 15) < 4) *(GAS f32x4*)(ws + WS_F + (fidx * 640 + (lane & 3) * 160 + (w * 10 + e) * 4 + (lane >> 4)) * 4) = d;
      }
    }
    WO_STEP(4); WO_STEP(5);
#undef WO_STEP
    wg_barrier();
    if (tid < 4) {
      const f32x4 mm = *(LAS f32x4*)(lds + L_M + tid * 8), mh = *(LAS f32x4*)(lds + L_M + tid * 8 + 4), ll = *(LAS f32x4*)(lds + L_L + tid * 4);
      *(GAS f32x2*)(ws + WS_ML + (tid * 256 + vec) * 2) = f32x2{fmaxf(fmaxf(fmaxf(mm[0], mm[1]), fmaxf(mm[2], mm[3])), fmaxf(fmaxf(mh[0], mh[1]), fmaxf(mh[2], mh[3]))), (ll[0] + ll[1]) + (ll[2] + ll[3])};
    }
  }
  wg_barrier();
  {
    u32x4 ob[6];
#pragma unroll
    for (int ks = 0; ks < 6; ++ks) ob[ks] = *(LAS u32x4*)(lds + L_Q + (lane & 15) * OSTR + ks * 16 + (lane >> 4) * 4);
    f32x4 y[5];
#pragma unroll
    for (int e = 0; e < 5; ++e) y[e] = f32x4{0.f, 0.f, 0.f, 0.f};
#pragma unroll
    for (int ks = 0; ks < 6; ++ks)
#pragma unroll
      for (int e = 0; e < 5; ++e) y[e] = mfma(wf[ks][e], ob[ks], y[e]);
    GAS float* yp = (GAS float*)ws + WS_Y + ((((lane >> 2) & 3) * 160 + ((w & 3) * 10 + (w >> 2) * 5) * 4 + (lane >> 4)) * 256 + vec) * 4;
#pragma unroll
    for (int e = 0; e < 5; ++e) {
#pragma unroll
      for (int k = 0; k < 4; ++k) { float t = y[e][k]; t += dpp<XOR1>(t); t += dpp<XOR2>(t); y[e][k] = t; }
      if ((lane & 3) == 0) *(GAS f32x4*)(yp + e * (4 * 256 * 4)) = y[e];
    }
  }
}

__global__ void __launch_bounds__(256)
k2_mix(const float* __restrict__ ws, unsigned short* __restrict__ out) {
  const int lane = threadIdx.x & 63, task = blockIdx.x * 4 + __builtin_amdgcn_readfirstlane(threadIdx.x >> 6);
  const int b = task / 160, g4 = task - b * 160;
  f32x4 y[4];
  f32x2 ml[4];
#pragma unroll
  for (int i = 0; i < 4; ++i) {
    y[i] = *(const GAS f32x4*)(ws + WS_Y + (task * 256 + i * 64 + lane) * 4);
    ml[i] = *(const GAS f32x2*)(ws + WS_ML + (b * 256 + i * 64 + lane) * 2);
  }
  f32x4 f = *(const GAS f32x4*)(ws + WS_F + ((lane < 48 ? lane : 0) * 640 + task) * 4);
  const float g = ws[WS_G + b];
  SB();
  if (lane >= 48) f = f32x4{0.f, 0.f, 0.f, 0.f};
  f32x4 a = {0.f, 0.f, 0.f, 0.f};
#pragma unroll
  for (int i = 0; i < 4; ++i) {
    const float e = __builtin_amdgcn_exp2f(ml[i][0] - max8(ml[i][0]));
    a += (e * __builtin_amdgcn_rcpf(sum8(e * ml[i][1]))) * y[i];
  }
  float v[8] = {a[0], a[1], a[2], a[3], f[0], f[1], f[2], f[3]};
  slot_reduce<8>(v, lane);
  const float t = rbf(sum8(v[0]));
  auto sw = __builtin_amdgcn_permlane32_swap(__float_as_uint(t), __float_as_uint(t), false, false);
  const float o = rbf(g * __uint_as_float(sw[0])) + rbf(rbf(1.f - g) * __uint_as_float(sw[1]));
  const int c = g4 >> 2, i = c / 10, e = c - i * 10, m = (g4 & 3) * 4 + (lane >> 3);
  const int col = 160 * i + (e < 6 ? 6 * m + e : 90 + 4 * m + e);
  if (lane < 32 && (lane & 7) == 0) out[b * D + col] = f2bf(o);
}
}

extern "C" void kernel_launch(void* const* d_in, const int* in_sizes, int n_in, void* d_out, int out_size, void* d_ws,
                              size_t ws_size, hipStream_t stream) {
  const unsigned short* x = (const unsigned short*)d_in[0];
  const unsigned short* kc = (const unsigned short*)d_in[1];
  const unsigned short* vc = (const unsigned short*)d_in[2];
  const unsigned short* wq = (const unsigned short*)d_in[3];
  const unsigned short* wo = (const unsigned short*)d_in[4];
  const unsigned short* wg = (const unsigned short*)d_in[6];
  const unsigned short* wu = (const unsigned short*)d_in[7];
  const unsigned short* wd = (const unsigned short*)d_in[8];
  const unsigned short* gw = (const unsigned short*)d_in[9];
  float* ws = (float*)d_ws;
  hipLaunchKernelGGL(k1_attn, dim3(256), dim3(NT), 0, stream, x, kc, vc, wq, wo, wg, wu, wd, gw, ws);
  hipLaunchKernelGGL(k2_mix, dim3(160), dim3(256), 0, stream, (const float*)ws, (unsigned short*)d_out);
}
```

```cpp
#include <hip/hip_runtime.h>
#include <hip/hip_bf16.h>
#include <stdint.h>

#define GAS __attribute__((address_space(1)))
#define LAS __attribute__((address_space(3)))
typedef unsigned u32x4 __attribute__((ext_vector_type(4)));
typedef unsigned u32x3 __attribute__((ext_vector_type(3)));
typedef unsigned u32x2 __attribute__((ext_vector_type(2)));
typedef float f32x4 __attribute__((ext_vector_type(4)));
typedef float f32x2 __attribute__((ext_vector_type(2)));
typedef __bf16 bf16x2 __attribute__((ext_vector_type(2)));
typedef __bf16 bf16x8 __attribute__((ext_vector_type(8)));
typedef short s16x4 __attribute__((ext_vector_type(4)));

namespace {
constexpr int H = 32, SK = 8192, DH = 192, D = 640, DF = 1536;
constexpr int NT = 512;
constexpr int ROWB = DH * 2;
constexpr int TILE_B = 8 * ROWB;
constexpr int RA = 5, RB = 722;
constexpr int T0 = 300, TGE = 331, TGO = 329, XS1 = 22, XS2 = 18;
constexpr int LAG = 4;
constexpr int WS_Y = 0;
constexpr int WS_F = 4 * 160 * 256 * 4;
constexpr int WS_ML = WS_F + 4 * 160 * 48 * 4;
constexpr int WS_G = WS_ML + 4 * 256 * 2;

__device__ __forceinline__ float dot2(unsigned a, unsigned b, float acc) {
  return __builtin_amdgcn_fdot2_f32_bf16(__builtin_bit_cast(bf16x2, a), __builtin_bit_cast(bf16x2, b), acc, false);
}
__device__ __forceinline__ f32x4 mfma(u32x4 a, u32x4 b, f32x4 c) {
  return __builtin_amdgcn_mfma_f32_16x16x32_bf16(__builtin_bit_cast(bf16x8, a), __builtin_bit_cast(bf16x8, b), c, 0, 0, 0);
}
template <int CTRL> __device__ __forceinline__ float dpp(float x) {
  return __builtin_bit_cast(float, __builtin_amdgcn_mov_dpp(__builtin_bit_cast(int, x), CTRL, 0xf, 0xf, true));
}
constexpr int XOR1 = 0xB1, XOR2 = 0x4E, HMIR = 0x141, ROR8 = 0x128;
__device__ __forceinline__ float bflo(unsigned u) { return __uint_as_float(u << 16); }
__device__ __forceinline__ float bfhi(unsigned u) { return __uint_as_float(u & 0xffff0000u); }
__device__ __forceinline__ float bf2f(unsigned short u) { return __uint_as_float((unsigned)u << 16); }
__device__ __forceinline__ unsigned short f2bf(float f) { return __builtin_bit_cast(unsigned short, (__bf16)f); }
__device__ __forceinline__ float rbf(float f) { return bf2f(f2bf(f)); }
__device__ __forceinline__ unsigned pk2(float lo, float hi) { return (unsigned)f2bf(lo) | ((unsigned)f2bf(hi) << 16); }
__device__ __forceinline__ unsigned plo(unsigned r0, unsigned r1) { return __builtin_amdgcn_perm(r1, r0, 0x05040100u); }
__device__ __forceinline__ unsigned phi(unsigned r0, unsigned r1) { return __builtin_amdgcn_perm(r1, r0, 0x07060302u); }
__device__ __forceinline__ float swap32_add(float a, float b) {
  auto r = __builtin_amdgcn_permlane32_swap(__float_as_uint(a), __float_as_uint(b), false, false);
  return __uint_as_float(r[0]) + __uint_as_float(r[1]);
}
__device__ __forceinline__ float swap16_add(float a, float b) {
  auto r = __builtin_amdgcn_permlane16_swap(__float_as_uint(a), __float_as_uint(b), false, false);
  return __uint_as_float(r[0]) + __uint_as_float(r[1]);
}
__device__ __forceinline__ float max8(float x) { x = fmaxf(x, dpp<XOR1>(x)); x = fmaxf(x, dpp<XOR2>(x)); return fmaxf(x, dpp<HMIR>(x)); }
__device__ __forceinline__ float sum8(float x) { x += dpp<XOR1>(x); x += dpp<XOR2>(x); return x + dpp<HMIR>(x); }
__device__ __forceinline__ float wave_max(float x) {
  x = max8(x);
  x = fmaxf(x, dpp<ROR8>(x));
  auto s = __builtin_amdgcn_permlane16_swap(__float_as_uint(x), __float_as_uint(x), false, false);
  x = fmaxf(__uint_as_float(s[0]), __uint_as_float(s[1]));
  auto t = __builtin_amdgcn_permlane32_swap(__float_as_uint(x), __float_as_uint(x), false, false);
  return fmaxf(__uint_as_float(t[0]), __uint_as_float(t[1]));
}
__device__ __forceinline__ float wave_sum(float x) {
  x = sum8(x);
  x = x + dpp<ROR8>(x);
  x = swap16_add(x, x);
  return swap32_add(x, x);
}
template <int N> __device__ __forceinline__ void slot_reduce(float (&v)[N], int lane) {
#pragma unroll
  for (int j = 0; j < N / 2; ++j) v[j] = swap32_add(v[j], v[j + N / 2]);
#pragma unroll
  for (int j = 0; j < N / 4; ++j) v[j] = swap16_add(v[j], v[j + N / 4]);
#pragma unroll
  for (int j = 0; j < N / 8; ++j) {
    const float a = v[j] + dpp<ROR8>(v[j]), b = v[j + N / 8] + dpp<ROR8>(v[j + N / 8]);
    v[j] = (lane & 8) ? b : a;
  }
}
template <int AUX> __device__ __forceinline__ u32x4 bload(__amdgpu_buffer_rsrc_t r, int voff, int soff = 0) {
  return __builtin_amdgcn_raw_buffer_load_b128(r, voff, soff, AUX);
}
__device__ __forceinline__ __amdgpu_buffer_rsrc_t mkrsrc(const void* p, unsigned bytes) {
  return __builtin_amdgcn_make_buffer_rsrc((void*)p, 0, (int)bytes, 0x00020000);
}
#define SB() __builtin_amdgcn_sched_barrier(0)
#define PIN24(a, o) asm volatile("" : "+v"(a[o + 0]), "+v"(a[o + 1]), "+v"(a[o + 2]), "+v"(a[o + 3]), "+v"(a[o + 4]), "+v"(a[o + 5]), "+v"(a[o + 6]), "+v"(a[o + 7]), \
  "+v"(a[o + 8]), "+v"(a[o + 9]), "+v"(a[o + 10]), "+v"(a[o + 11]), "+v"(a[o + 12]), "+v"(a[o + 13]), "+v"(a[o + 14]), "+v"(a[o + 15]), \
  "+v"(a[o + 16]), "+v"(a[o + 17]), "+v"(a[o + 18]), "+v"(a[o + 19]), "+v"(a[o + 20]), "+v"(a[o + 21]), "+v"(a[o + 22]), "+v"(a[o + 23]) :: "memory")
__device__ __forceinline__ void wg_barrier() {
  asm volatile("s_waitcnt lgkmcnt(0)\n\ts_barrier" ::: "memory");
}

struct RowPair { u32x3 a0, a1; u32x2 b0, b1; };
__device__ __forceinline__ RowPair frag_load(__amdgpu_buffer_rsrc_t r, int va, int vb, int soff) {
  RowPair p;
  p.a0 = __builtin_amdgcn_raw_buffer_load_b96(r, va, soff, 0);
  p.b0 = __builtin_amdgcn_raw_buffer_load_b64(r, vb, soff, 0);
  p.a1 = __builtin_amdgcn_raw_buffer_load_b96(r, va + D * 2, soff, 0);
  p.b1 = __builtin_amdgcn_raw_buffer_load_b64(r, vb + D * 2, soff, 0);
  return p;
}
__device__ __forceinline__ unsigned frag_dword(const RowPair& p, int e) {
  if (e < 6) return (e & 1) ? phi(p.a0[e >> 1], p.a1[e >> 1]) : plo(p.a0[e >> 1], p.a1[e >> 1]);
  return (e & 1) ? phi(p.b0[(e - 6) >> 1], p.b1[(e - 6) >> 1]) : plo(p.b0[(e - 6) >> 1], p.b1[(e - 6) >> 1]);
}

constexpr int L_W = 0;
constexpr int L_S = 4 * 30 * 256;
constexpr int L_Q = L_S + 4 * 1024;
constexpr int OSTR = 100;
constexpr int L_G = L_Q + 4 * 4 * 256;
constexpr int L_M = L_G + 4 * 256;
constexpr int L_L = L_M + 32;
constexpr int L_X = L_S - 4 * 320;
constexpr int L_QN = L_L + 16;
constexpr int L_END = L_QN + 4 * 96;

#define LOAD_Q1(dst, i, bb) do { \
          f32x4 a0 = {0.f, 0.f, 0.f, 0.f}, a1 = {0.f, 0.f, 0.f, 0.f}; \
          _Pragma("unroll") for (int hw = 0; hw < ((i) < 2 ? 4 : 8); ++hw) { \
            a0 += *(LAS f32x4*)(lds + L_Q + ((hw & 3) * 4 + (bb)) * 256 + ((i) + (hw >> 2)) * 64 + c8 * 8); \
            a1 += *(LAS f32x4*)(lds + L_Q + ((hw & 3) * 4 + (bb)) * 256 + ((i) + (hw >> 2)) * 64 + c8 * 8 + 4); \
            if (hw & 1) { asm volatile("" : "+v"(a0), "+v"(a1) :: "memory"); } } \
          dst = u32x4{pk2(a0[0], a0[1]), pk2(a0[2], a0[3]), pk2(a1[0], a1[1]), pk2(a1[2], a1[3])}; } while (0)
#define TREDUCE(xs) do { \
        _Pragma("unroll") for (int t = 0; t < 4; ++t) { const float keep = (c8 & 4) ? xs[t + 4] : xs[t], send = (c8 & 4) ? xs[t] : xs[t + 4]; xs[t] = keep + dpp<HMIR>(send); } \
        _Pragma("unroll") for (int t = 0; t < 2; ++t) { const float keep = (c8 & 2) ? xs[t + 2] : xs[t], send = (c8 & 2) ? xs[t] : xs[t + 2]; xs[t] = keep + dpp<XOR2>(send); } \
        { const float keep = (c8 & 1) ? xs[1] : xs[0], send = (c8 & 1) ? xs[0] : xs[1]; xs[0] = keep + dpp<XOR1>(send); } } while (0)
constexpr float C2 = 0.07216878364870322f * 1.4426950408889634f;

__global__ void __launch_bounds__(NT, 2)
k1_attn(const unsigned short* __restrict__ x, const unsigned short* __restrict__ kc, const unsigned short* __restrict__ vc,
        const unsigned short* __restrict__ wq, const unsigned short* __restrict__ wo, const unsigned short* __restrict__ wg,
        const unsigned short* __restrict__ wu, const unsigned short* __restrict__ wd, const unsigned short* __restrict__ gw,
        float* __restrict__ ws) {
  __shared__ __attribute__((aligned(16))) float lds_[L_END];
  LAS float* lds = (LAS float*)lds_;
  const int tid = threadIdx.x, lane = tid & 63, w = __builtin_amdgcn_readfirstlane(tid >> 6);
  const int rs = lane >> 3, c8 = lane & 7;
  const int bid = blockIdx.x, xg = bid & 7, j = bid >> 3;
  const int h = xg * 4 + (j & 3), mem = j >> 2;
  const int vec = h * 8 + mem;
  const int rel = 6 * (j & 3);
  u32x4 wf[6][5];

  if (w >= 4) {
    const int sw = w - 4;
    const unsigned tstart = (unsigned)__builtin_amdgcn_s_memrealtime() + ((mem * 4 + sw) * 84) / 32 - 42 + T0;
    const int TG = (xg & 1) ? TGO : TGE;
#define PACE(k, t) do { if ((t) & 1) break; const int n_ = (k) * 8 + (t); \
      const int lim = (n_ * TG + min(max(n_ - 64, 0), 32) * XS1 + min(max(n_ - 160, 0), 32) * XS2) >> 3; \
      while ((int)((unsigned)__builtin_amdgcn_s_memrealtime() - tstart) < lim) __builtin_amdgcn_s_sleep(2); \
      SB(); } while (0)
    const __amdgpu_buffer_rsrc_t rk = mkrsrc(kc, 4u * H * SK * ROWB);
    const __amdgpu_buffer_rsrc_t rv = mkrsrc(vc, 4u * H * SK * ROWB);
    const int loff = rs * ROWB + c8 * 16;
    const int t0 = xg * RA + (j & 3) * RB + (mem * 4 + sw) * 2;
    u32x4 rb[16][3];
#define VR(r) ((r) ^ 6)
#define ISSUE_G(u, rsrc, gi) do { const int so = ((((gi) >> 2) * H + h) * 1024 + ((t0 + (((gi) & 3) * 8 + ((u) & 6)) * 32 + ((u) & 1)) & 1023)) * TILE_B; \
      _Pragma("unroll") for (int i = 0; i < 3; ++i) { rb[u][i] = bload<2>(rsrc, loff + i * 128, so); SB(); } } while (0)
#define ISSUE(u, rsrc, r) ISSUE_G(u, rsrc, (r) * 2 + ((u) >> 3))
#pragma unroll
    for (int u = 8; u < 16; ++u) { ISSUE_G(u, rk, 0); if (rel == u - 7) wg_barrier(); }
#pragma unroll
    for (int u = 0; u < 8; ++u) { ISSUE_G(u, rk, 1); if (rel == u + 9) wg_barrier(); }
#pragma unroll
    for (int u = 8; u < 16; ++u) {
#pragma unroll
      for (int i = 0; i < 3; ++i) *(LAS u32x4*)(lds + L_W + (sw * 30 + (u - 8) * 3 + i) * 256 + lane * 4) = rb[u][i];
      asm volatile("" ::: "memory");
      SB();
      ISSUE_G(u, rk, 2);
      if (rel == u + 9) wg_barrier();
    }
    wg_barrier();
    LAS float* scw = lds + L_S + sw * 1024;
    u32x4 qf[3];
    LAS u32x4* qn = (LAS u32x4*)(lds + L_QN + sw * 96) + c8;
#pragma unroll
    for (int i = 0; i < 3; ++i) LOAD_Q1(qf[i], i, 0);
    float m = -INFINITY;
#define K_TILE(u, dst) do { float s0 = 0.f, s1 = 0.f; \
          _Pragma("unroll") for (int i = 0; i < 3; ++i) { \
            s0 = dot2(rb[u][i][0], qf[i][0], s0); s1 = dot2(rb[u][i][1], qf[i][1], s1); \
            s0 = dot2(rb[u][i][2], qf[i][2], s0); s1 = dot2(rb[u][i][3], qf[i][3], s1); } \
          s0 += s1; \
          asm volatile("" : "+v"(s0) :: "memory"); \
          SB(); \
          dst = s0; } while (0)
#define K_SCORES(xs, gi) do { TREDUCE(xs); const float sc = xs[0] * C2; m = fmaxf(m, sc); scw[(gi) * 64 + lane] = sc; asm volatile("" : "+v"(m) :: "memory"); SB(); } while (0)
#define K_GROUP(g, gi, PRE, rn, gn, kn) do { float xs[8]; \
        _Pragma("unroll") for (int t = 0; t < 8; ++t) { \
          K_TILE((g) * 8 + t, xs[t]); \
          PACE(kn, t); \
          ISSUE_G((g) * 8 + t, rn, gn); \
          if (PRE && (t & 1) == 0 && t < 6) { u32x4 qq; LOAD_Q1(qq, t >> 1, (((gi) + 1) >> 2) & 3); qn[(t >> 1) * 8] = qq; asm volatile("" ::: "memory"); SB(); } \
        } \
        K_SCORES(xs, gi); } while (0)
    const __amdgpu_buffer_rsrc_t rz = mkrsrc(vc, 0);
#pragma unroll 1
    for (int r = 0; r < 8; ++r) {
      const __amdgpu_buffer_rsrc_t ra = r == 7 ? rv : rk, rb2 = r == 6 ? rz : rk;
      K_GROUP(0, 2 * r + 1, true, ra, r == 7 ? VR(0) * 2 : 2 * r + 3, r == 7 ? 16 : 2 * r + 3);
      if (r & 1) {
        const float mw = wave_max(m);
        if (lane == 0) lds[L_M + (r >> 1) * 8 + sw] = mw;
        m = -INFINITY;
#pragma unroll
        for (int i = 0; i < 3; ++i) qf[i] = qn[i * 8];
      }
      if (r == 7) break;
      K_GROUP(1, 2 * r + 2, false, rb2, (2 * r + 4) & 15, r == 6 ? 0 : 2 * r + 4);
    }
#undef K_GROUP
#undef K_SCORES
#undef K_TILE
    PACE(17, 0);
    ISSUE(8, rv, VR(0));
    wg_barrier();
#pragma unroll
    for (int t = 1; t < 8; ++t) {
      if (t < 5) {
        const int bb = t - 1;
        const f32x4 mm = *(LAS f32x4*)(lds + L_M + bb * 8), mh = *(LAS f32x4*)(lds + L_M + bb * 8 + 4);
        const float M = fmaxf(fmaxf(fmaxf(mm[0], mm[1]), fmaxf(mm[2], mm[3])), fmaxf(fmaxf(mh[0], mh[1]), fmaxf(mh[2], mh[3])));
        float l = 0.f;
#pragma unroll
        for (int g = 0; g < 4; ++g) {
          const float p = __builtin_amdgcn_exp2f(scw[(bb * 4 + g) * 64 + lane] - M);
          l += p;
          scw[(bb * 4 + g) * 64 + lane] = p;
        }
        l = wave_sum(l);
        if (lane == 0) lds[L_L + bb * 4 + sw] = l;
        asm volatile("" ::: "memory");
        SB();
      }
      PACE(17, t);
      ISSUE(8 + t, rv, VR(0));
    }
    float acc[24];
#pragma unroll
    for (int k = 0; k < 24; ++k) acc[k] = 0.f;
#define V_TILE(u, p) do { \
          _Pragma("unroll") for (int i = 0; i < 3; ++i) \
            _Pragma("unroll") for (int e = 0; e < 4; ++e) { \
              acc[i * 8 + 2 * e] = fmaf(p, bflo(rb[u][i][e]), acc[i * 8 + 2 * e]); \
              acc[i * 8 + 2 * e + 1] = fmaf(p, bfhi(rb[u][i][e]), acc[i * 8 + 2 * e + 1]); } \
          PIN24(acc, 0); \
          SB(); } while (0)
#define V_ROUND(r) do { \
      _Pragma("unroll") for (int g = 0; g < 2; ++g) { \
        const f32x4 p0 = *(LAS f32x4*)(scw + (VR(r) * 2 + g) * 64 + rs * 8), p1 = *(LAS f32x4*)(scw + (VR(r) * 2 + g) * 64 + rs * 8 + 4); \
        _Pragma("unroll") for (int t = 0; t < 8; ++t) { \
          V_TILE(g * 8 + t, (t < 4 ? p0[t & 3] : p1[t & 3])); \
          PACE(18 + 2 * (r) + g, t); \
          ISSUE(g * 8 + t, rv, VR((r) + 1)); \
        } } } while (0)
#define V_FLUSH(bb) do { \
      slot_reduce<24>(acc, lane); \
      LAS unsigned short* ob = (LAS unsigned short*)(lds + L_Q + ((bb) * 4 + sw) * OSTR); \
      _Pragma("unroll") for (int jj = 0; jj < 3; ++jj) { const int k = 3 * rs + jj; ob[(k >> 3) * 64 + c8 * 8 + (k & 7)] = f2bf(acc[jj]); } } while (0)
#pragma unroll 1
    for (int r = 0; r < 6; ++r) {
      V_ROUND(r);
      if (r & 1) {
        V_FLUSH(VR(r) >> 1);
#pragma unroll
        for (int k = 0; k < 24; ++k) acc[k] = 0.f;
      }
    }
    wg_barrier();
    {
      f32x4 pq;
#pragma unroll
      for (int c = 0; c < 32; ++c) {
        if ((c & 3) == 0) pq = *(LAS f32x4*)(scw + (VR(6) * 2 + (c >> 3)) * 64 + rs * 8 + (c & 4));
        V_TILE(c & 15, pq[c & 3]);
        if (c >= 16 && c < 26) {
#pragma unroll
          for (int q = 0; q < 3; ++q) { const int f = (c - 16) * 3 + q; wf[f / 5][f % 5] = *(LAS u32x4*)(lds + L_W + (sw * 30 + f) * 256 + lane * 4); }
          SB();
        }
        const int n = c < 2 * (16 - LAG) ? ((c & 1) ? c >> 1 : 99) : c - (16 - LAG);
        if (n < 16) { PACE(30 + (n >> 3), n & 7); ISSUE(n & 15, rv, VR(7)); }
      }
    }
    V_FLUSH(VR(7) >> 1);
    wg_barrier();
#undef V_TILE
#undef V_ROUND
#undef V_FLUSH
#undef ISSUE
#undef ISSUE_G
#undef PACE
#undef VR
  } else {
    __builtin_amdgcn_s_setprio(1);
    {
      const __amdgpu_buffer_rsrc_t rq = mkrsrc(wq, D * H * DH * 2);
      const __amdgpu_buffer_rsrc_t rx = mkrsrc(x, 4 * D * 2);
      const int mm = lane & 15, gg = lane >> 4;
      const int qa = (160 * w + gg * 8) * (H * DH * 2) + h * ROWB + mm * 16;
      const int qb = (160 * w + (mm >> 3) * 16 + gg * 4) * (H * DH * 2) + h * ROWB + 256 + (mm & 7) * 16;
      const int xa = ((lane & 3) * D + 160 * w + gg * 8) * 2, xc = ((lane & 3) * D + 160 * w + ((mm >> 2) & 1) * 16 + gg * 4) * 2;
      const int k0 = mem >= 5 ? mem - 5 : mem;
      constexpr int QR = 4;
      u32x4 wr[QR][12], x32[QR];
      u32x2 x16[QR];
#define Q_ISSUE(s_) do { int ks = (s_) + k0; ks = ks >= 5 ? ks - 5 : ks; \
        x32[(s_) % QR] = bload<0>(rx, xa, ks * 64); SB(); \
        x16[(s_) % QR] = __builtin_amdgcn_raw_buffer_load_b64(rx, xc, ks * 64, 0); SB(); \
        _Pragma("unroll") for (int r = 0; r < 8; ++r) { wr[(s_) % QR][r] = bload<0>(rq, qa, (ks * 32 + r) * (H * DH * 2)); SB(); } \
        _Pragma("unroll") for (int r = 0; r < 4; ++r) { wr[(s_) % QR][8 + r] = bload<0>(rq, qb, (ks * 32 + r) * (H * DH * 2)); SB(); } } while (0)
      if (rel) wg_barrier();
#pragma unroll
      for (int s = 0; s < QR; ++s) Q_ISSUE(s);
      f32x4 a[6];
#pragma unroll
      for (int e = 0; e < 6; ++e) a[e] = f32x4{0.f, 0.f, 0.f, 0.f};
#pragma unroll
      for (int s = 0; s < 5; ++s) {
        const u32x4* wp = wr[s % QR];
#pragma unroll
        for (int c = 0; c < 4; ++c) {
          const u32x4 bm = (mm >> 2) == c ? x32[s % QR] : u32x4{0u, 0u, 0u, 0u};
#pragma unroll
          for (int ai = 0; ai < 2; ++ai) {
            const int e = ai * 4 + c;
            u32x4 af;
#pragma unroll
            for (int t = 0; t < 4; ++t) af[t] = (e & 1) ? phi(wp[2 * t][e >> 1], wp[2 * t + 1][e >> 1]) : plo(wp[2 * t][e >> 1], wp[2 * t + 1][e >> 1]);
            a[ai] = mfma(af, bm, a[ai]);
          }
        }
#pragma unroll
        for (int c = 0; c < 2; ++c) {
          const u32x2 bm = (mm >> 3) == c ? x16[s % QR] : u32x2{0u, 0u};
#pragma unroll
          for (int ai = 0; ai < 4; ++ai) {
            const u32x2 af = c ? u32x2{phi(wp[8][ai], wp[9][ai]), phi(wp[10][ai], wp[11][ai])} : u32x2{plo(wp[8][ai], wp[9][ai]), plo(wp[10][ai], wp[11][ai])};
            a[2 + ai] = __builtin_amdgcn_mfma_f32_16x16x16bf16_1k(__builtin_bit_cast(s16x4, af), __builtin_bit_cast(s16x4, bm), a[2 + ai], 0, 0, 0);
          }
        }
        asm volatile("" : "+v"(a[0]), "+v"(a[1]), "+v"(a[2]), "+v"(a[3]), "+v"(a[4]), "+v"(a[5]) :: "memory");
        SB();
        if (s + QR < 5) Q_ISSUE(s + QR);
      }
#undef Q_ISSUE
      LAS float* qp = lds + L_Q + (w * 4 + (lane & 3)) * 256;
#pragma unroll
      for (int ai = 0; ai < 2; ++ai)
#pragma unroll
        for (int r = 0; r < 4; ++r) qp[8 * (4 * gg + r) + 4 * ai + (mm >> 2)] = a[ai][r];
      if ((gg >> 1) == ((mm >> 2) & 1)) {
#pragma unroll
        for (int ai = 0; ai < 4; ++ai)
#pragma unroll
          for (int r = 0; r < 4; ++r) qp[128 + 8 * (4 * gg + r) + 2 * ai + (mm >> 3)] = a[2 + ai][r];
      }
    }
    wg_barrier();
    {
      u32x4 qf[3];
#pragma unroll
      for (int i = 0; i < 3; ++i) LOAD_Q1(qf[i], i, 0);
      float xs[8];
#pragma unroll
      for (int t = 0; t < 8; ++t) {
        float s0 = 0.f, s1 = 0.f;
#pragma unroll
        for (int i = 0; i < 3; ++i) {
          const u32x4 kk = *(LAS u32x4*)(lds + L_W + (w * 30 + t * 3 + i) * 256 + lane * 4);
          s0 = dot2(kk[0], qf[i][0], s0); s1 = dot2(kk[1], qf[i][1], s1);
          s0 = dot2(kk[2], qf[i][2], s0); s1 = dot2(kk[3], qf[i][3], s1);
        }
        xs[t] = s0 + s1;
      }
      TREDUCE(xs);
      const float sc = xs[0] * C2;
      lds[L_S + w * 1024 + lane] = sc;
      const float mw = wave_max(sc);
      if (lane < 4) lds[L_M + lane * 8 + 4 + w] = lane == 0 ? mw : -INFINITY;
    }
    const __amdgpu_buffer_rsrc_t ro = mkrsrc(wo + (size_t)h * DH * D, DH * D * 2);
    {
      u32x4 t[8];
#pragma unroll
      for (int i = 0; i < 8; ++i) t[i] = bload<0>(ro, (i == 7 && lane >= 32) ? 0x40000000 : mem * 30720 + w * 7680 + i * 1024 + lane * 16);
#pragma unroll
      for (int i = 0; i < 8; ++i) asm volatile("" :: "v"(t[i]) : "memory");
      SB();
    }
    const bool ffn = (xg & 1) == 0 && j < 12;
    const int fidx = (xg >> 1) * 12 + j;
    if ((xg & 1) == 0 && j >= 12) {
#pragma unroll 1
      for (int i0 = 0; i0 < 6; i0 += 2) {
        u32x4 t[4];
#pragma unroll
        for (int i = 0; i < 2; ++i) {
          const int o = ((j - 12) * 32 + w * 8 + rs) * (DF * 2) + (xg >> 1) * 768 + (i0 + i) * 128 + c8 * 16;
          t[i] = bload<0>(mkrsrc(wg, D * DF * 2), o);
          t[2 + i] = bload<0>(mkrsrc(wu, D * DF * 2), o);
        }
#pragma unroll
        for (int i = 0; i < 4; ++i) asm volatile("" :: "v"(t[i]) : "memory");
      }
      {
        u32x4 t[6];
#pragma unroll
        for (int i = 0; i < 6; ++i) t[i] = bload<0>(mkrsrc(wd, D * DF * 2), (xg >> 1) * 491520 + (j - 12) * 24576 + w * 6144 + i * 1024 + lane * 16);
#pragma unroll
        for (int i = 0; i < 6; ++i) asm volatile("" :: "v"(t[i]) : "memory");
      }
    }
    float gate = 0.f;
    if (bid == 254) {
      const GAS unsigned short* xb_ = (const GAS unsigned short*)x + w * D;
      const u32x4 xa = *(const GAS u32x4*)(xb_ + lane * 8), ga = *(const GAS u32x4*)((const GAS unsigned short*)gw + lane * 8);
      const int l2 = 64 + (lane & 15);
      const u32x4 xb = *(const GAS u32x4*)(xb_ + l2 * 8), gb = *(const GAS u32x4*)((const GAS unsigned short*)gw + l2 * 8);
      float gs = 0.f, gs2 = 0.f;
#pragma unroll
      for (int e = 0; e < 4; ++e) {
        gs += rbf(bflo(xa[e]) * bflo(ga[e])) + rbf(bfhi(xa[e]) * bfhi(ga[e]));
        gs2 += rbf(bflo(xb[e]) * bflo(gb[e])) + rbf(bfhi(xb[e]) * bfhi(gb[e]));
      }
      gs = wave_sum(gs + (lane < 16 ? gs2 : 0.f));
      gate = rbf(1.f / (1.f + __expf(-rbf(gs))));
    }
    const int va = (lane >> 4) * 8 * (D * 2) + 320 * w + (lane & 15) * 12, vb = (lane >> 4) * 8 * (D * 2) + 320 * w + 192 + (lane & 15) * 8;
#define WO_STEP(ks) do { u32x4 sf[5]; \
      _Pragma("unroll") for (int t = 0; t < 4; ++t) { \
        const RowPair p = frag_load(ro, va, vb, ((ks) * 32 + 2 * t) * (D * 2)); \
        _Pragma("unroll") for (int e = 0; e < 5; ++e) { wf[ks][e][t] = frag_dword(p, e); sf[e][t] = frag_dword(p, 5 + e); } \
        asm volatile("" : "+v"(wf[ks][0]), "+v"(wf[ks][1]), "+v"(wf[ks][2]), "+v"(wf[ks][3]), "+v"(wf[ks][4]), \
                          "+v"(sf[0]), "+v"(sf[1]), "+v"(sf[2]), "+v"(sf[3]), "+v"(sf[4]) :: "memory"); \
        SB(); } \
      _Pragma("unroll") for (int e = 0; e < 5; ++e) *(LAS u32x4*)(lds + L_W + (w * 30 + (ks) * 5 + e) * 256 + lane * 4) = sf[e]; } while (0)
    WO_STEP(0); WO_STEP(1);
    if (ffn) {
      LAS unsigned* xl = (LAS unsigned*)lds + L_X + w * 320;
      {
        const u32x4 xs0 = *(const GAS u32x4*)((const GAS unsigned short*)x + (lane / 20) * D + 160 * w + (lane % 20) * 8);
        const u32x4 xs1 = *(const GAS u32x4*)((const GAS unsigned short*)x + 3 * D + 160 * w + (4 + (lane & 15)) * 8);
        *(LAS u32x4*)(xl + lane * 4) = xs0;
        if (lane < 16) *(LAS u32x4*)(xl + 256 + lane * 4) = xs1;
      }
      const GAS unsigned short* gp = (const GAS unsigned short*)(c8 < 4 ? wg : wu) + (size_t)(160 * w + 2 * rs) * DF + fidx * 32 + (c8 & 3) * 8;
      float ga[32];
#pragma unroll
      for (int k = 0; k < 32; ++k) ga[k] = 0.f;
#pragma unroll 1
      for (int g = 0; g < 5; ++g) {
        u32x4 t[4];
#pragma unroll
        for (int r = 0; r < 4; ++r) t[r] = *(const GAS u32x4*)(gp + (size_t)(g * 32 + (r >> 1) * 16 + (r & 1)) * DF);
#pragma unroll
        for (int r = 0; r < 2; ++r) {
          unsigned xx[4];
#pragma unroll
          for (int bb = 0; bb < 4; ++bb) xx[bb] = xl[bb * 80 + g * 16 + r * 8 + rs];
#pragma unroll
          for (int e = 0; e < 4; ++e) {
            const unsigned lo = plo(t[2 * r][e], t[2 * r + 1][e]), hi = phi(t[2 * r][e], t[2 * r + 1][e]);
#pragma unroll
            for (int bb = 0; bb < 4; ++bb) {
              ga[bb * 8 + 2 * e] = dot2(lo, xx[bb], ga[bb * 8 + 2 * e]);
              ga[bb * 8 + 2 * e + 1] = dot2(hi, xx[bb], ga[bb * 8 + 2 * e + 1]);
            }
          }
        }
      }
      slot_reduce<32>(ga, lane);
      *(LAS f32x4*)(lds + L_G + w * 256 + (rs >> 1) * 64 + c8 * 8 + (rs & 1) * 4) = f32x4{ga[0], ga[1], ga[2], ga[3]};
    }
    WO_STEP(2);
    WO_STEP(3);
    wg_barrier();
    f32x4 fd[10];
    if (ffn) {
      const __amdgpu_buffer_rsrc_t rd = mkrsrc(wd + (size_t)fidx * 32 * D, 32 * D * 2);
      RowPair p[4];
#pragma unroll
      for (int t = 0; t < 4; ++t) p[t] = frag_load(rd, va, vb, 2 * t * (D * 2));
      float hv[8];
      const int go = L_G + (lane & 3) * 64 + (lane >> 4) * 8;
#pragma unroll
      for (int q = 0; q < 2; ++q) {
        f32x4 g4 = {0.f, 0.f, 0.f, 0.f}, u4 = {0.f, 0.f, 0.f, 0.f};
#pragma unroll
        for (int hw = 0; hw < 4; ++hw) { g4 += *(LAS f32x4*)(lds + go + hw * 256 + q * 4); u4 += *(LAS f32x4*)(lds + go + hw * 256 + 32 + q * 4); }
#pragma unroll
        for (int e = 0; e < 4; ++e) {
          const float g = rbf(g4[e]), u = rbf(u4[e]);
          const float inner = 0.7978845608028654f * (g + 0.044715f * g * g * g);
          hv[q * 4 + e] = rbf(g / (1.f + __expf(-2.f * inner))) * u;
        }
      }
      const u32x4 hb = {pk2(hv[0], hv[1]), pk2(hv[2], hv[3]), pk2(hv[4], hv[5]), pk2(hv[6], hv[7])};
#pragma unroll
      for (int e = 0; e < 10; ++e) {
        const u32x4 af = {frag_dword(p[0], e), frag_dword(p[1], e), frag_dword(p[2], e), frag_dword(p[3], e)};
        fd[e] = mfma(af, hb, f32x4{0.f, 0.f, 0.f, 0.f});
        asm volatile("" : "+v"(fd[e]));
      }
    }
    WO_STEP(4); WO_STEP(5);
#undef WO_STEP
    wg_barrier();
    f32x2 mlv;
    {
      const int bb = lane & 3;
      const f32x4 mm = *(LAS f32x4*)(lds + L_M + bb * 8), mh = *(LAS f32x4*)(lds + L_M + bb * 8 + 4), ll = *(LAS f32x4*)(lds + L_L + bb * 4);
      mlv = f32x2{fmaxf(fmaxf(fmaxf(mm[0], mm[1]), fmaxf(mm[2], mm[3])), fmaxf(fmaxf(mh[0], mh[1]), fmaxf(mh[2], mh[3]))), (ll[0] + ll[1]) + (ll[2] + ll[3])};
      asm volatile("" : "+v"(mlv), "+v"(gate));
    }
    wg_barrier();
    if (tid < 4) *(GAS f32x2*)(ws + WS_ML + (tid * 256 + vec) * 2) = mlv;
    if (ffn) {
#pragma unroll
      for (int e = 0; e < 10; ++e)
        if ((lane & 15) < 4) *(GAS f32x4*)(ws + WS_F + (fidx * 640 + (lane & 3) * 160 + (w * 10 + e) * 4 + (lane >> 4)) * 4) = fd[e];
    }
    if (bid == 254 && lane == 0) ws[WS_G + w] = gate;
  }
  {
    u32x4 ob[6];
#pragma unroll
    for (int ks = 0; ks < 6; ++ks) ob[ks] = *(LAS u32x4*)(lds + L_Q + (lane & 15) * OSTR + ks * 16 + (lane >> 4) * 4);
    f32x4 y[5];
#pragma unroll
    for (int e = 0; e < 5; ++e) y[e] = f32x4{0.f, 0.f, 0.f, 0.f};
#pragma unroll
    for (int ks = 0; ks < 6; ++ks)
#pragma unroll
      for (int e = 0; e < 5; ++e) y[e] = mfma(wf[ks][e], ob[ks], y[e]);
    GAS float* yp = (GAS float*)ws + WS_Y + ((((lane >> 2) & 3) * 160 + ((w & 3) * 10 + (w >> 2) * 5) * 4 + (lane >> 4)) * 256 + vec) * 4;
#pragma unroll
    for (int e = 0; e < 5; ++e) {
#pragma unroll
      for (int k = 0; k < 4; ++k) { float t = y[e][k]; t += dpp<XOR1>(t); t += dpp<XOR2>(t); y[e][k] = t; }
      if ((lane & 3) == 0) *(GAS f32x4*)(yp + e * (4 * 256 * 4)) = y[e];
    }
  }
}

__global__ void __launch_bounds__(256)
k2_mix(const float* __restrict__ ws, unsigned short* __restrict__ out) {
  const int lane = threadIdx.x & 63, task = blockIdx.x * 4 + __builtin_amdgcn_readfirstlane(threadIdx.x >> 6);
  const int b = task / 160, g4 = task - b * 160;
  asm volatile("" :: "s"(out));
  f32x4 y[4];
  f32x2 ml[4];
#pragma unroll
  for (int i = 0; i < 4; ++i) { ml[i] = *(const GAS f32x2*)(ws + WS_ML + (b * 256 + i * 64 + lane) * 2); SB(); }
#pragma unroll
  for (int i = 0; i < 4; ++i) { y[i] = *(const GAS f32x4*)(ws + WS_Y + (task * 256 + i * 64 + lane) * 4); SB(); }
  f32x4 f = *(const GAS f32x4*)(ws + WS_F + ((lane < 48 ? lane : 0) * 640 + task) * 4);
  const float g = __uint_as_float(__builtin_amdgcn_raw_buffer_load_b32(mkrsrc(ws + WS_G, 16), b * 4, 0, 0));
  asm volatile("" ::: "memory");
  SB();
  if (lane >= 48) f = f32x4{0.f, 0.f, 0.f, 0.f};
  f32x4 a = {0.f, 0.f, 0.f, 0.f};
#pragma unroll
  for (int i = 0; i < 4; ++i) {
    const float e = __builtin_amdgcn_exp2f(ml[i][0] - max8(ml[i][0]));
    a += (e * __builtin_amdgcn_rcpf(sum8(e * ml[i][1]))) * y[i];
  }
  float v[8] = {a[0], a[1], a[2], a[3], f[0], f[1], f[2], f[3]};
  slot_reduce<8>(v, lane);
  const float t = rbf(sum8(v[0]));
  auto sw = __builtin_amdgcn_permlane32_swap(__float_as_uint(t), __float_as_uint(t), false, false);
  const float o = rbf(g * __uint_as_float(sw[0])) + rbf(rbf(1.f - g) * __uint_as_float(sw[1]));
  const int c = g4 >> 2, i = c / 10, e = c - i * 10, m = (g4 & 3) * 4 + (lane >> 3);
  const int col = 160 * i + (e < 6 ? 6 * m + e : 90 + 4 * m + e);
  if (lane < 32 && (lane & 7) == 0) out[b * D + col] = f2bf(o);
}
}

extern "C" void kernel_launch(void* const* d_in, const int* in_sizes, int n_in, void* d_out, int out_size, void* d_ws,
                              size_t ws_size, hipStream_t stream) {
  const unsigned short* x = (const unsigned short*)d_in[0];
  const unsigned short* kc = (const unsigned short*)d_in[1];
  const unsigned short* vc = (const unsigned short*)d_in[2];
  const unsigned short* wq = (const unsigned short*)d_in[3];
  const unsigned short* wo = (const unsigned short*)d_in[4];
  const unsigned short* wg = (const unsigned short*)d_in[6];
  const unsigned short* wu = (const unsigned short*)d_in[7];
  const unsigned short* wd = (const unsigned short*)d_in[8];
  const unsigned short* gw = (const unsigned short*)d_in[9];
  float* ws = (float*)d_ws;
  hipLaunchKernelGGL(k1_attn, dim3(256), dim3(NT), 0, stream, x, kc, vc, wq, wo, wg, wu, wd, gw, ws);
  hipLaunchKernelGGL(k2_mix, dim3(160), dim3(256), 0, stream, (const float*)ws, (unsigned short*)d_out);
}
```
